```python
import torch

def _softmax(x, axis=-1):
    return torch.softmax(x, dim=axis)
def _gelu(x):
    return torch.nn.functional.gelu(x, approximate="tanh")
def _silu(x):
    return torch.nn.functional.silu(x)
def _sigmoid(x):
    return torch.sigmoid(x)
def _layernorm(x, axis=-1, eps=1e-5):
    m = x.mean(dim=axis, keepdim=True)
    v = x.var(dim=axis, keepdim=True, unbiased=False)
    return (x - m) / torch.sqrt(v + eps)
def _scatter_add(tgt, idx, src, axis=0):
    return tgt.clone().index_add_(axis, idx, src)
def _segment_sum(src, seg, n):
    out = torch.zeros((n,) + src.shape[1:], dtype=src.dtype)
    return out.index_add_(0, seg, src)
def _pad(x, pw):
    flat = tuple(v for pair in reversed(pw) for v in pair)
    return torch.nn.functional.pad(x, flat)

kernel_name = 'parallel_sum__sliding_block_cached__mixer_channel_mlp__decode'

def setup_inputs():

    _w = (lambda a: (lambda t: t.to(torch.bfloat16) if t.is_floating_point() else t)(torch.as_tensor(a)))
    def _A_setup():
        B, Sq, D = 1, 1, 576
        Hq, Hkv, Sk, Dh = 32, 16, 65536, 128
        assert Hq % Hkv == 0, (Hq, Hkv)
        return {
            "x": _w((lambda *s: torch.randn(*s, dtype=torch.bfloat16) * 0.05)(B, Sq, D)),
            "k_cache": _w((lambda *s: torch.randn(*s, dtype=torch.bfloat16) * 3.2)(B, Hkv, Sk, Dh)),
            "v_cache": _w((lambda *s: torch.randn(*s, dtype=torch.bfloat16) * 0.4)(B, Hkv, Sk, Dh)),
            "wq": _w((lambda *s: torch.randn(*s, dtype=torch.bfloat16) * 3.2)(D, Hq * Dh) * (D ** -0.5)),
            "wo": _w((lambda *s: torch.randn(*s, dtype=torch.bfloat16) * 0.4)(Hq * Dh, D) * ((Hq * Dh) ** -0.5)),
        }
    _A_in = _A_setup()

    def _B_setup():
        B, S, D, F = 1, 1, 576, 4608
        return {
            "x": _w((lambda *s: torch.randn(*s, dtype=torch.bfloat16) * 0.05)(B, S, D)),
            "w1": _w((lambda *s: torch.randn(*s, dtype=torch.bfloat16) * 3.2)(D, F) * (D ** -0.5)),
            "b1": _w((lambda *s: torch.randn(*s, dtype=torch.bfloat16) * 0.8)(F)),
            "w2": _w((lambda *s: torch.randn(*s, dtype=torch.bfloat16) * 0.1)(F, D) * (F ** -0.5)),
            "b2": _w((lambda *s: torch.randn(*s, dtype=torch.bfloat16) * 0.1)(D)),
        }
    _B_in = _B_setup()

    return {
        "A_x": _A_in["x"],
        "A_k_cache": _A_in["k_cache"],
        "A_v_cache": _A_in["v_cache"],
        "A_wq": _A_in["wq"],
        "A_wo": _A_in["wo"],
        "B_x": _B_in["x"],
        "B_w1": _B_in["w1"],
        "B_b1": _B_in["b1"],
        "B_w2": _B_in["w2"],
        "B_b2": _B_in["b2"],
    }

def reference(A_x, A_k_cache, A_v_cache, A_wq, A_wo, B_x, B_w1, B_b1, B_w2, B_b2):
    def _A_ref(x, k_cache, v_cache, wq, wo):
        B, Sq, D = x.shape
        Hq, Dh = 32, 128
        Hkv, Sk = k_cache.shape[1], k_cache.shape[2]
        W = min(4096, max(256, Sk // 4))
        rep = Hq // Hkv
        q = (lambda x, *d: x.permute(*d))((x @ wq).reshape(B, Sq, Hq, Dh), 0, 2, 1, 3)
        k = (lambda x, n, axis: torch.repeat_interleave(x, n, dim=axis))(k_cache[:, :, Sk - W:, :], rep, axis=1)
        v = (lambda x, n, axis: torch.repeat_interleave(x, n, dim=axis))(v_cache[:, :, Sk - W:, :], rep, axis=1)
        scores = torch.einsum("bhqd,bhkd->bhqk", q, k) * (Dh ** -0.5)
        p = _softmax(scores, axis=-1)
        o = torch.einsum("bhqk,bhkd->bhqd", p, v)
        return (lambda x, *d: x.permute(*d))(o, 0, 2, 1, 3).reshape(B, Sq, Hq * Dh) @ wo

    def _B_ref(x, w1, b1, w2, b2):
        y = _layernorm(x, axis=-1)
        h = _gelu(torch.einsum("bsd,df->bsf", y, w1) + b1)
        return torch.einsum("bsf,fd->bsd", h, w2) + b2

    x = A_x
    xn = _layernorm(x, axis=-1)
    return x + _A_ref(xn, A_k_cache, A_v_cache, A_wq, A_wo) + _B_ref(xn, B_w1, B_b1, B_w2, B_b2)
```

```cpp
#include <hip/hip_runtime.h>
#include <hip/hip_bf16.h>
#include <cstdint>

typedef unsigned short u16;
typedef unsigned v4u __attribute__((ext_vector_type(4)));

constexpr int D = 576, HQ = 32, HKV = 16, SK = 65536, DH = 128, W = 4096, F = 4608;
constexpr int NSPLIT = 16, KPS = W / NSPLIT;

__device__ __forceinline__ float bf2f(u16 v) { return __uint_as_float((unsigned)v << 16); }
__device__ __forceinline__ float rbf(float f) {
  unsigned u = __float_as_uint(f);
  u = (u + 0x7fffu + ((u >> 16) & 1u)) & 0xffff0000u;
  return __uint_as_float(u);
}
__device__ __forceinline__ u16 f2bf(float f) { return (u16)(__float_as_uint(rbf(f)) >> 16); }

__device__ float block_sum(float v, float* red) {
  for (int o = 32; o > 0; o >>= 1) v += __shfl_xor(v, o);
  int w = threadIdx.x >> 6;
  __syncthreads();
  if ((threadIdx.x & 63) == 0) red[w] = v;
  __syncthreads();
  float s = 0;
  for (int i = 0; i < (int)(blockDim.x >> 6); ++i) s += red[i];
  return s;
}

__device__ void ln_bf16(const float* src, float* dst, float* red) {
  float s = 0;
  for (int i = threadIdx.x; i < D; i += blockDim.x) s += src[i];
  float mean = block_sum(s, red) / D;
  float s2 = 0;
  for (int i = threadIdx.x; i < D; i += blockDim.x) { float d = src[i] - mean; s2 += d * d; }
  float var = rbf(block_sum(s2, red) / D);
  float m = rbf(mean);
  float den = rbf(sqrtf(rbf(var + 1e-5f)));
  for (int i = threadIdx.x; i < D; i += blockDim.x) dst[i] = rbf(rbf(src[i] - m) / den);
  __syncthreads();
}

__global__ void __launch_bounds__(256) k1(const u16* x, const u16* wq, const u16* w1, const u16* b1, float* qout, float* hout) {
  __shared__ float xs[D], xn[D], y[D], red[8], part[8][64];
  for (int i = threadIdx.x; i < D; i += 256) xs[i] = bf2f(x[i]);
  __syncthreads();
  ln_bf16(xs, xn, red);
  ln_bf16(xn, y, red);
  int b = blockIdx.x;
  bool isq = b < 64;
  const u16* Wm = isq ? wq : w1;
  int N = isq ? 4096 : F;
  int c0 = (isq ? b : b - 64) * 64;
  const float* act = isq ? xn : y;
  int c = threadIdx.x & 31, r = threadIdx.x >> 5;
  float a0 = 0, a1 = 0;
  for (int d = r; d < D; d += 8) {
    unsigned w = *(const unsigned*)(Wm + (size_t)d * N + c0 + 2 * c);
    a0 += act[d] * __uint_as_float(w << 16);
    a1 += act[d] * __uint_as_float(w & 0xffff0000u);
  }
  part[r][2 * c] = a0; part[r][2 * c + 1] = a1;
  __syncthreads();
  if (threadIdx.x < 64) {
    float s = 0;
    for (int i = 0; i < 8; ++i) s += part[i][threadIdx.x];
    s = rbf(s);
    if (isq) qout[c0 + threadIdx.x] = s;
    else {
      float t = rbf(s + bf2f(b1[c0 + threadIdx.x]));
      float g = 0.5f * t * (1.f + tanhf(0.7978845608028654f * (t + 0.044715f * t * t * t)));
      hout[c0 + threadIdx.x] = rbf(g);
    }
  }
}

__global__ void __launch_bounds__(256) k2(const u16* kc, const u16* vc, const float* q, float* pm, float* po) {
  __shared__ float qs[2][DH], p[2][KPS], red[8], oacc[4][2][DH];
  int s = blockIdx.x, g = blockIdx.y, t = threadIdx.x;
  qs[t >> 7][t & 127] = q[g * 256 + t];
  __syncthreads();
  const u16* kb = kc + ((size_t)g * SK + (SK - W) + s * KPS) * DH;
  const u16* vb = vc + ((size_t)g * SK + (SK - W) + s * KPS) * DH;
  float s0 = 0, s1 = 0;
  for (int i = 0; i < DH; i += 8) {
    v4u kv = *(const v4u*)(kb + (size_t)t * DH + i);
    for (int j = 0; j < 4; ++j) {
      float lo = __uint_as_float(kv[j] << 16), hi = __uint_as_float(kv[j] & 0xffff0000u);
      s0 += lo * qs[0][i + 2 * j] + hi * qs[0][i + 2 * j + 1];
      s1 += lo * qs[1][i + 2 * j] + hi * qs[1][i + 2 * j + 1];
    }
  }
  s0 = rbf(rbf(s0) * 0.08838834764831845f);
  s1 = rbf(rbf(s1) * 0.08838834764831845f);
  float m0 = s0, m1 = s1;
  for (int o = 32; o > 0; o >>= 1) { m0 = fmaxf(m0, __shfl_xor(m0, o)); m1 = fmaxf(m1, __shfl_xor(m1, o)); }
  if ((t & 63) == 0) { red[t >> 6] = m0; red[4 + (t >> 6)] = m1; }
  __syncthreads();
  m0 = fmaxf(fmaxf(red[0], red[1]), fmaxf(red[2], red[3]));
  m1 = fmaxf(fmaxf(red[4], red[5]), fmaxf(red[6], red[7]));
  float e0 = __expf(s0 - m0), e1 = __expf(s1 - m1);
  p[0][t] = e0; p[1][t] = e1;
  float l0 = block_sum(e0, red);
  float l1 = block_sum(e1, red);
  __syncthreads();
  int dd = 2 * (t & 63), kg = t >> 6;
  float o00 = 0, o01 = 0, o10 = 0, o11 = 0;
  for (int k = kg; k < KPS; k += 4) {
    unsigned w = *(const unsigned*)(vb + (size_t)k * DH + dd);
    float lo = __uint_as_float(w << 16), hi = __uint_as_float(w & 0xffff0000u);
    o00 += p[0][k] * lo; o01 += p[0][k] * hi; o10 += p[1][k] * lo; o11 += p[1][k] * hi;
  }
  oacc[kg][0][dd] = o00; oacc[kg][0][dd + 1] = o01; oacc[kg][1][dd] = o10; oacc[kg][1][dd + 1] = o11;
  __syncthreads();
  int hh = t >> 7, d = t & 127;
  float o = oacc[0][hh][d] + oacc[1][hh][d] + oacc[2][hh][d] + oacc[3][hh][d];
  int head = g * 2 + hh;
  po[((size_t)head * NSPLIT + s) * DH + d] = o;
  if (d == 0) { pm[(head * NSPLIT + s) * 2] = hh ? m1 : m0; pm[(head * NSPLIT + s) * 2 + 1] = hh ? l1 : l0; }
}

__global__ void __launch_bounds__(256) k3(const u16* x, const u16* wo, const u16* w2, const u16* b2, const float* pm, const float* po, const float* h, u16* out) {
  __shared__ float o[4096], wgt[HQ][NSPLIT], partA[32][16], partB[32][16];
  int t = threadIdx.x;
  if (t < HQ) {
    float M = -INFINITY;
    for (int s = 0; s < NSPLIT; ++s) M = fmaxf(M, pm[(t * NSPLIT + s) * 2]);
    float L = 0;
    for (int s = 0; s < NSPLIT; ++s) L += __expf(pm[(t * NSPLIT + s) * 2] - M) * pm[(t * NSPLIT + s) * 2 + 1];
    for (int s = 0; s < NSPLIT; ++s) wgt[t][s] = __expf(pm[(t * NSPLIT + s) * 2] - M) / L;
  }
  __syncthreads();
  for (int i = t; i < 4096; i += 256) {
    int head = i >> 7, d = i & 127;
    float a = 0;
    for (int s = 0; s < NSPLIT; ++s) a += wgt[head][s] * po[((size_t)head * NSPLIT + s) * DH + d];
    o[i] = rbf(a);
  }
  __syncthreads();
  int c0 = blockIdx.x * 16, c = t & 7, r = t >> 3;
  float a0 = 0, a1 = 0, b0 = 0, b1_ = 0;
  for (int k = r; k < 4096; k += 32) {
    unsigned w = *(const unsigned*)(wo + (size_t)k * D + c0 + 2 * c);
    a0 += o[k] * __uint_as_float(w << 16); a1 += o[k] * __uint_as_float(w & 0xffff0000u);
  }
  for (int k = r; k < F; k += 32) {
    unsigned w = *(const unsigned*)(w2 + (size_t)k * D + c0 + 2 * c);
    float hv = h[k];
    b0 += hv * __uint_as_float(w << 16); b1_ += hv * __uint_as_float(w & 0xffff0000u);
  }
  partA[r][2 * c] = a0; partA[r][2 * c + 1] = a1; partB[r][2 * c] = b0; partB[r][2 * c + 1] = b1_;
  __syncthreads();
  if (t < 16) {
    float A = 0, Bv = 0;
    for (int i = 0; i < 32; ++i) { A += partA[i][t]; Bv += partB[i][t]; }
    A = rbf(A); Bv = rbf(rbf(Bv) + bf2f(b2[c0 + t]));
    float r0 = rbf(bf2f(x[c0 + t]) + A);
    out[c0 + t] = f2bf(r0 + Bv);
  }
}

extern "C" void kernel_launch(void* const* d_in, const int* in_sizes, int n_in,
                              void* d_out, int out_size, void* d_ws, size_t ws_size,
                              hipStream_t stream) {
  const u16* x = (const u16*)d_in[0];
  const u16* kc = (const u16*)d_in[1];
  const u16* vc = (const u16*)d_in[2];
  const u16* wq = (const u16*)d_in[3];
  const u16* wo = (const u16*)d_in[4];
  const u16* w1 = (const u16*)d_in[6];
  const u16* b1 = (const u16*)d_in[7];
  const u16* w2 = (const u16*)d_in[8];
  const u16* b2 = (const u16*)d_in[9];
  float* ws = (float*)d_ws;
  float* q = ws; float* h = ws + 4096; float* pm = ws + 16384; float* po = ws + 32768;
  k1<<<64 + 72, 256, 0, stream>>>(x, wq, w1, b1, q, h);
  k2<<<dim3(NSPLIT, HKV), 256, 0, stream>>>(kc, vc, q, pm, po);
  k3<<<36, 256, 0, stream>>>(x, wo, w2, b2, pm, po, h, (u16*)d_out);
}
```

```cpp
#include <hip/hip_runtime.h>
#include <hip/hip_bf16.h>
#include <cstdint>

typedef unsigned short u16;
typedef unsigned v4u __attribute__((ext_vector_type(4)));
typedef unsigned v2u __attribute__((ext_vector_type(2)));
typedef float v4f __attribute__((ext_vector_type(4)));
typedef float v2f __attribute__((ext_vector_type(2)));
typedef __bf16 bf16x2 __attribute__((ext_vector_type(2)));
typedef short bf16x8 __attribute__((ext_vector_type(8)));
#define LAS __attribute__((address_space(3)))

#define WIN 4
#define WWIN 7
#define NOPOLL 0
#define ILV 1
#define KSLEEP 4
#define W1_AT 2
#define WF_AT 8
#define STAG 1
#define KVI_AT 7
#define CODE_KB 13
#define DLY_Q 30
#define DLY_O 16
#define DLY_H 16
#define DLY_P 16

constexpr int D = 576, SK = 65536, W = 4096, F = 4608;
constexpr int NTHR = 960;
constexpr unsigned OOB = 0x80000000u;
constexpr unsigned WS_E = 0;
constexpr unsigned WS_Q = 4096;
constexpr unsigned WS_H = WS_Q + 256 * 64 * 8;
constexpr unsigned WS_O = WS_H + 256 * 288 * 8;
constexpr unsigned WS_M = WS_O + 256 * 256 * 8;
constexpr unsigned WS_P = WS_M + 256 * 128;
constexpr unsigned WS_END = WS_P + 256 * 576 * 8;
constexpr unsigned WS_DBG = 8u << 20;
constexpr int L_XN = 0, L_Y = L_XN + 288, L_QB = L_Y + 288, L_MW = L_QB + 128, L_LW = L_MW + 16, L_CO = L_LW + 16,
              L_OP = L_CO + 32, L_FLAG = L_OP + 8 * 256, L_FR = L_FLAG + 32, L_FY = L_FR + 9 * 256, L_END = L_FY + 9 * 256;
constexpr int LDS_BYTES = 96 * 1024;
static_assert(L_END * 4 <= LDS_BYTES, "lds");
enum { F_CH = 11, F_W1I = 10, F_KVI = 9, F_Y = 0, F_QPUB, F_Q, F_MCNT, F_OCNT, F_HPUB, F_CO, F_PPUB, F_TAG };
constexpr unsigned SPIN_CAP = 1u << 22;
constexpr float LOG2E = 1.4426950408889634f;

__device__ __forceinline__ float rbf(float f) {
  unsigned u = __float_as_uint(f);
  u = (u + 0x7fffu + ((u >> 16) & 1u)) & 0xffff0000u;
  return __uint_as_float(u);
}
__device__ __forceinline__ float lo16(unsigned w) { return __uint_as_float(w << 16); }
__device__ __forceinline__ float hi16(unsigned w) { return __uint_as_float(w & 0xffff0000u); }
__device__ __forceinline__ float asf(unsigned w) { return __uint_as_float(w); }
__device__ __forceinline__ unsigned asu(float w) { return __float_as_uint(w); }
__device__ __forceinline__ unsigned pklo(unsigned a, unsigned b) { return __builtin_amdgcn_perm(b, a, 0x05040100u); }
__device__ __forceinline__ unsigned pkhi(unsigned a, unsigned b) { return __builtin_amdgcn_perm(b, a, 0x07060302u); }
__device__ __forceinline__ float dot2(unsigned k, unsigned q, float acc) {
  return __builtin_amdgcn_fdot2_f32_bf16(__builtin_bit_cast(bf16x2, k), __builtin_bit_cast(bf16x2, q), acc, false);
}
template <int CTRL> __device__ __forceinline__ float dpp(float x) {
  return __builtin_bit_cast(float, __builtin_amdgcn_mov_dpp(__builtin_bit_cast(int, x), CTRL, 0xf, 0xf, true));
}
template <int CTRL> __device__ __forceinline__ unsigned dppu(unsigned x) { return (unsigned)__builtin_amdgcn_mov_dpp((int)x, CTRL, 0xf, 0xf, true); }
constexpr int XOR1 = 0xB1, XOR2 = 0x4E, XOR3 = 0x1B, XOR7 = 0x141, XOR8 = 0x128, ROR4 = 0x124;
__device__ __forceinline__ float tr16(float a, float b) {
  auto s = __builtin_amdgcn_permlane16_swap(asu(a), asu(b), false, false);
  return asf(s[0]) + asf(s[1]);
}
__device__ __forceinline__ float tr32(float a, float b) {
  auto s = __builtin_amdgcn_permlane32_swap(asu(a), asu(b), false, false);
  return asf(s[0]) + asf(s[1]);
}
__device__ __forceinline__ float max16(float a) {
  auto s = __builtin_amdgcn_permlane16_swap(asu(a), asu(a), false, false);
  return fmaxf(asf(s[0]), asf(s[1]));
}
__device__ __forceinline__ float max32(float a) {
  auto s = __builtin_amdgcn_permlane32_swap(asu(a), asu(a), false, false);
  return fmaxf(asf(s[0]), asf(s[1]));
}
__device__ __forceinline__ float wave_sum(float t) {
  t += dpp<XOR1>(t); t += dpp<XOR2>(t); t += dpp<ROR4>(t); t += dpp<XOR8>(t);
  t = tr16(t, t); t = tr32(t, t);
  return t;
}
__device__ __forceinline__ float red8(float (&a)[8], int lane) {
#pragma unroll
  for (int h = 0; h < 4; ++h) { const float t1 = a[h] + dpp<XOR8>(a[h]), t2 = a[h + 4] + dpp<XOR8>(a[h + 4]); a[h] = (lane & 8) ? t2 : t1; }
  a[0] = tr16(a[0], a[2]); a[1] = tr16(a[1], a[3]);
  return tr32(a[0], a[1]);
}
#define VMWAIT(n) asm volatile("s_waitcnt vmcnt(%0)" :: "n"(n) : "memory")
#define LGKM0() asm volatile("s_waitcnt lgkmcnt(0)" ::: "memory")
#define LDNT(r, off) __builtin_amdgcn_raw_buffer_load_b128((r), (off), 0, 2)
#define LDNT16(r, off) __builtin_amdgcn_raw_buffer_load_b16((r), (off), 0, 2)
#define LDSC(r, off, so) __builtin_amdgcn_raw_buffer_load_b128((r), (off), (so), 16)
#define LDSC8(r, off, so) __builtin_amdgcn_raw_buffer_load_b64((r), (off), (so), 16)
#define STSC(v, r, off) __builtin_amdgcn_raw_buffer_store_b128((v), (r), (off), 0, 16)
#define STSC8(v, r, off) __builtin_amdgcn_raw_buffer_store_b64((v), (r), (off), 0, 16)
#define SB() __builtin_amdgcn_sched_barrier(0)
#define PIN8(a) asm volatile("" : "+v"(a[0]), "+v"(a[1]), "+v"(a[2]), "+v"(a[3]), "+v"(a[4]), "+v"(a[5]), "+v"(a[6]), "+v"(a[7]))

template <bool SLEEP> __device__ __forceinline__ void lds_wait_ge(volatile LAS unsigned* p, unsigned v) {
#pragma unroll 1
  for (unsigned n = 0; n < SPIN_CAP; ++n) { if (*p >= v) break; if (SLEEP) __builtin_amdgcn_s_sleep(1); }
  asm volatile("" ::: "memory");
}
__device__ __forceinline__ void lds_arrive(LAS unsigned* p, int lane) {
  asm volatile("" ::: "memory");
  if (lane == 0) (void)__hip_atomic_fetch_add(p, 1u, __ATOMIC_RELAXED, __HIP_MEMORY_SCOPE_WORKGROUP);
  asm volatile("" ::: "memory");
}

__device__ __forceinline__ void layernorm9(float (&v)[9]) {
  float s = ((v[0] + v[1]) + (v[2] + v[3])) + ((v[4] + v[5]) + (v[6] + v[7])) + v[8];
  const float mean = wave_sum(s) * (1.f / D);
  float s2 = 0.f, s3 = 0.f;
#pragma unroll
  for (int k = 0; k < 8; k += 2) { const float d = v[k] - mean, e = v[k + 1] - mean; s2 = fmaf(d, d, s2); s3 = fmaf(e, e, s3); }
  { const float d = v[8] - mean; s2 = fmaf(d, d, s2); }
  const float var = rbf(wave_sum(s2 + s3) * (1.f / D));
  const float mb = rbf(mean);
  const float den = rbf(__builtin_amdgcn_sqrtf(rbf(var + 1e-5f)));
  const float inv = __builtin_amdgcn_rcpf(den);
#pragma unroll
  for (int k = 0; k < 9; ++k) {
    const float n = rbf(v[k] - mb);
    float qv = n * inv;
    qv = fmaf(fmaf(-qv, den, n), inv, qv);
    v[k] = rbf(qv);
  }
}
__device__ __forceinline__ void store9(LAS unsigned* dst, const float (&v)[9], int lane) {
  *(LAS v4u*)(dst + 4 * lane) = v4u{pkhi(asu(v[0]), asu(v[1])), pkhi(asu(v[2]), asu(v[3])), pkhi(asu(v[4]), asu(v[5])), pkhi(asu(v[6]), asu(v[7]))};
  ((LAS u16*)dst)[512 + lane] = (u16)(asu(v[8]) >> 16);
}

#define WSTAMP(k) do {} while (0)
#define WDUMP() do {} while (0)

__global__ void __launch_bounds__(NTHR) fused_decode(const u16* __restrict__ x, const u16* __restrict__ kc, const u16* __restrict__ vc,
                                                     const u16* __restrict__ wq, const u16* __restrict__ wo, const u16* __restrict__ w1,
                                                     const u16* __restrict__ b1, const u16* __restrict__ w2, const u16* __restrict__ b2,
                                                     u16* __restrict__ out, unsigned char* ws) {
  unsigned long long pc0;
  asm volatile("s_getpc_b64 %0" : "=s"(pc0));
  extern __shared__ __attribute__((aligned(16))) float smem[];
  LAS float* L = (LAS float*)smem;
  LAS unsigned* LU = (LAS unsigned*)smem;
  volatile LAS unsigned* FL = (volatile LAS unsigned*)(LU + L_FLAG);
  const int t = threadIdx.x, lane = t & 63;
  const int wave = __builtin_amdgcn_readfirstlane(t >> 6);
  const int u = blockIdx.x, g = u >> 4, gi = u & 15;
  auto mk = [](const void* p, unsigned bytes) { return __builtin_amdgcn_make_buffer_rsrc((void*)p, 0, bytes, 0x00020000); };
  const auto rws = mk(ws, WS_END);
  const auto rx = mk(x, D * 2);

  if (wave == 0) {
    const int xcd = u & 7, jj = u >> 3;
    const int lg = (jj >> 2) * 8 + xcd;
    const bool is_red = lg < 36;
    const int rcol = 16 * lg + 4 * (jj & 3);
    const auto rc = mk((const void*)(pc0 & ~255ull), CODE_KB * 1024);
    const v4u cpf = __builtin_amdgcn_raw_buffer_load_b128(rc, (jj % CODE_KB) * 1024 + lane * 16, 0, 0);
    const auto rb2 = mk(b2, D * 2);
    const int oc = 2 * (lane & 1) + (lane >> 5);
    const unsigned xrr = __builtin_amdgcn_raw_buffer_load_b16(rx, is_red ? (rcol + oc) * 2 : OOB, 0, 0);
    const unsigned b2r = __builtin_amdgcn_raw_buffer_load_b16(rb2, is_red ? (rcol + oc) * 2 : OOB, 0, 0);
    if (lane < 32) LU[L_FLAG + lane] = 0u;
    LGKM0();
    __builtin_amdgcn_s_barrier();
    SB();
    asm volatile("" :: "v"(cpf));
    lds_wait_ge<true>(FL + F_QPUB, 1);
    const unsigned tag = __builtin_amdgcn_readfirstlane(FL[F_TAG]);
    __builtin_amdgcn_s_sleep(DLY_Q);
    WSTAMP(1);
    {
      v4u qv[8];
      const unsigned qoff = WS_Q + g * 8192 + lane * 16;
#pragma unroll 1
      for (unsigned n = 0; n < SPIN_CAP; ++n) {
        asm volatile("" ::: "memory");
        unsigned bad = 0;
#pragma unroll
        for (int k = 0; k < 8; ++k) qv[k] = LDSC(rws, (NOPOLL & 1) ? OOB : qoff, k * 1024);
#pragma unroll
        for (int k = 0; k < 8; ++k) bad |= (qv[k].y ^ tag) | (qv[k].w ^ tag);
        if (__all(bad == 0) || (NOPOLL & 1)) break;
      }
      float s[8];
#pragma unroll
      for (int a = 0; a < 4; ++a) { s[2 * a] = asf(qv[2 * a].x) + asf(qv[2 * a + 1].x); s[2 * a + 1] = asf(qv[2 * a].z) + asf(qv[2 * a + 1].z); }
      const float r0 = tr32(s[0], s[4]), r1_ = tr32(s[1], s[5]), r2_ = tr32(s[2], s[6]), r3 = tr32(s[3], s[7]);
      const int a0 = (lane >> 5) * 2;
      LU[L_QB + a0 * 32 + (lane & 31)] = pkhi(asu(rbf(r0)), asu(rbf(r1_)));
      LU[L_QB + (a0 + 1) * 32 + (lane & 31)] = pkhi(asu(rbf(r2_)), asu(rbf(r3)));
      asm volatile("" ::: "memory");
      if (lane == 0) FL[F_Q] = 1u;
      WSTAMP(2);
    }
    lds_wait_ge<false>(FL + F_OCNT, 8);
    WSTAMP(3);
    {
      v2f oa = *(const LAS v2f*)(L + L_OP + 2 * lane), ob = *(const LAS v2f*)(L + L_OP + 128 + 2 * lane);
#pragma unroll
      for (int w = 1; w < 8; ++w) { oa += *(const LAS v2f*)(L + L_OP + w * 256 + 2 * lane); ob += *(const LAS v2f*)(L + L_OP + w * 256 + 128 + 2 * lane); }
      const int hd = lane & 1;
      const v4f la = *(const LAS v4f*)(L + L_LW + hd * 8), lb = *(const LAS v4f*)(L + L_LW + hd * 8 + 4);
      const v4f ma = *(const LAS v4f*)(L + L_MW + hd * 8), mb = *(const LAS v4f*)(L + L_MW + hd * 8 + 4);
      const float Ls = ((la.x + la.y) + (la.z + la.w)) + ((lb.x + lb.y) + (lb.z + lb.w));
      const float M = fmaxf(fmaxf(fmaxf(ma.x, ma.y), fmaxf(ma.z, ma.w)), fmaxf(fmaxf(mb.x, mb.y), fmaxf(mb.z, mb.w)));
      STSC((v4u{asu(oa.x), tag, asu(oa.y), tag}), rws, WS_O + u * 2048 + lane * 16);
      STSC((v4u{asu(ob.x), tag, asu(ob.y), tag}), rws, WS_O + u * 2048 + 1024 + lane * 16);
      STSC((v4u{asu(M), tag, asu(Ls), tag}), rws, lane < 8 ? WS_M + u * 128 + lane * 16 : OOB);
    }
    WSTAMP(4);
    __builtin_amdgcn_s_sleep(DLY_O);
    {
      const unsigned ooff = WS_O + (16 * g + (lane >> 4)) * 2048 + (16 * gi + (lane & 15)) * 8;
      const unsigned moff = WS_M + (16 * g + (lane >> 4)) * 128 + (gi >> 3) * 16;
      v2u ov[4]; v4u ml[4];
#pragma unroll 1
      for (unsigned n = 0; n < SPIN_CAP; ++n) {
        asm volatile("" ::: "memory");
        unsigned bad = 0;
#pragma unroll
        for (int k = 0; k < 4; ++k) { ov[k] = LDSC8(rws, (NOPOLL & 2) ? OOB : ooff, k * 8192); ml[k] = LDSC(rws, (NOPOLL & 2) ? OOB : moff, k * 512); }
#pragma unroll
        for (int k = 0; k < 4; ++k) bad |= (ov[k].y ^ tag) | (ml[k].y ^ tag) | (ml[k].w ^ tag);
        if (__all(bad == 0) || (NOPOLL & 2)) break;
      }
      WSTAMP(5);
      float M = fmaxf(fmaxf(asf(ml[0].x), asf(ml[1].x)), fmaxf(asf(ml[2].x), asf(ml[3].x)));
      M = max16(M); M = max32(M);
      float num = 0.f, den = 0.f;
#pragma unroll
      for (int k = 0; k < 4; ++k) { const float sc = __builtin_amdgcn_exp2f((asf(ml[k].x) - M) * LOG2E); num = fmaf(sc, asf(ov[k].x), num); den = fmaf(sc, asf(ml[k].z), den); }
      num = tr16(num, num); den = tr16(den, den); num = tr32(num, num); den = tr32(den, den);
      const float o = rbf(num * __builtin_amdgcn_rcpf(den));
      {
        const int d = lane & 15, m0 = 2 * (lane >> 4);
        LAS u16* fp = (LAS u16*)(LU + L_FR) + ((d >> 2) * 64 + (d & 3) * 16 + m0) * 8 + m0;
        fp[0] = (u16)(asu(o) >> 16); fp[9] = (u16)(asu(o) >> 16);
      }
      asm volatile("" ::: "memory");
      if (lane == 0) FL[F_CO] = 1u;
      WSTAMP(6);
    }
    if (is_red) {
      const unsigned off = WS_P + ((lane >> 1) * 576 + rcol + 2 * (lane & 1)) * 8;
      const float xb2 = lo16(xrr) + lo16(b2r);
      lds_wait_ge<false>(FL + F_PPUB, 1);
      __builtin_amdgcn_s_sleep(DLY_P);
      v4u pv[8];
#pragma unroll 1
      for (unsigned n = 0; n < SPIN_CAP; ++n) {
        asm volatile("" ::: "memory");
        unsigned bad = 0;
#pragma unroll
        for (int k = 0; k < 8; ++k) pv[k] = LDSC(rws, (NOPOLL & 4) ? OOB : off, k * (32 * 576 * 8));
#pragma unroll
        for (int k = 0; k < 8; ++k) bad |= (pv[k].y ^ tag) | (pv[k].w ^ tag);
        if (__all(bad == 0) || (NOPOLL & 4)) break;
      }
      float s0 = ((asf(pv[0].x) + asf(pv[1].x)) + (asf(pv[2].x) + asf(pv[3].x))) + ((asf(pv[4].x) + asf(pv[5].x)) + (asf(pv[6].x) + asf(pv[7].x)));
      float s1 = ((asf(pv[0].z) + asf(pv[1].z)) + (asf(pv[2].z) + asf(pv[3].z))) + ((asf(pv[4].z) + asf(pv[5].z)) + (asf(pv[6].z) + asf(pv[7].z)));
      s0 = tr32(s0, s1);
      s0 += dpp<XOR2>(s0); s0 += dpp<ROR4>(s0); s0 += dpp<XOR8>(s0); s0 = tr16(s0, s0);
      if ((lane & 30) == 0) out[rcol + oc] = (u16)(asu(rbf(s0 + xb2)) >> 16);
      if (u == 0 && lane == 0) __builtin_amdgcn_raw_buffer_store_b32(tag, rws, WS_E, 0, 16);
      WSTAMP(7);
    }
    WDUMP();
    return;
  }
  if (wave == 10) {
    const auto rb1 = mk(b1, F * 2);
    const unsigned b1r = LDNT16(rb1, (lane & 31) < 18 ? (18 * u + (lane & 31)) * 2 : OOB);
    __builtin_amdgcn_s_barrier();
    lds_wait_ge<true>(FL + F_QPUB, 1);
    const unsigned tag = __builtin_amdgcn_readfirstlane(FL[F_TAG]);
    {
      const v4u xa = *(const LAS v4u*)(LU + L_XN + 4 * lane);
      const unsigned xb = ((const LAS u16*)(LU + L_XN))[512 + lane];
      float v[9];
#pragma unroll
      for (int k = 0; k < 4; ++k) { v[2 * k] = lo16(xa[k]); v[2 * k + 1] = hi16(xa[k]); }
      v[8] = lo16(xb);
      layernorm9(v);
      store9(LU + L_Y, v, lane);
#pragma unroll
      for (int it = 0; it < 5; ++it) {
        const int idx = it * 64 + lane, n = idx < 288 ? idx >> 3 : 35, m = idx & 7;
        ((LAS u16*)(LU + L_FY))[((n >> 2) * 64 + (n & 3) * 16 + m) * 8 + m] = ((const LAS u16*)(LU + L_Y))[36 * gi + n];
      }
    }
    asm volatile("" ::: "memory");
    if (lane == 0) FL[F_Y] = 1u;
    WSTAMP(1);
    const unsigned hoff = (lane & 31) < 18 ? WS_H + ((16 * g + (lane >> 5)) * 288 + 18 * gi + (lane & 31)) * 8 : OOB;
    const unsigned tagx = (lane & 31) < 18 ? tag : 0u;
    lds_wait_ge<true>(FL + F_HPUB, 3);
    __builtin_amdgcn_s_sleep(DLY_H);
    WSTAMP(2);
    v2u hv[8];
#pragma unroll 1
    for (unsigned n = 0; n < SPIN_CAP; ++n) {
      asm volatile("" ::: "memory");
      unsigned bad = 0;
#pragma unroll
      for (int k = 0; k < 8; ++k) hv[k] = LDSC8(rws, (NOPOLL & 2) ? OOB : hoff, k * (2 * 288 * 8));
#pragma unroll
      for (int k = 0; k < 8; ++k) bad |= hv[k].y ^ tagx;
      if (__all(bad == 0) || (NOPOLL & 2)) break;
    }
    WSTAMP(3);
    float pre = ((asf(hv[0].x) + asf(hv[1].x)) + (asf(hv[2].x) + asf(hv[3].x))) + ((asf(hv[4].x) + asf(hv[5].x)) + (asf(hv[6].x) + asf(hv[7].x)));
    pre = tr32(pre, pre);
    const float tt = rbf(rbf(pre) + lo16(b1r));
    const float inner = 0.7978845608028654f * (tt + 0.044715f * tt * tt * tt);
    const float th = 1.f - 2.f * __builtin_amdgcn_rcpf(__builtin_amdgcn_exp2f(inner * (2.f * LOG2E)) + 1.f);
    const float h = rbf(0.5f * tt * (1.f + th));
    if ((lane & 31) < 18) {
      const int n = 16 + (lane & 31), m0 = 4 * (lane >> 5);
      LAS u16* fp = (LAS u16*)(LU + L_FR) + ((n >> 2) * 64 + (n & 3) * 16 + m0) * 8 + m0;
      fp[0] = (u16)(asu(h) >> 16); fp[9] = (u16)(asu(h) >> 16); fp[18] = (u16)(asu(h) >> 16); fp[27] = (u16)(asu(h) >> 16);
    }
    asm volatile("" ::: "memory");
    if (lane == 0) FL[F_CH] = 1u;
    WSTAMP(4);
    WDUMP();
    return;
  }

  if (wave >= 2 && wave < 10) {
    const int ww = wave - 2;
    const size_t kvb = ((size_t)g * SK + (SK - W) + gi * 256) * 128;
    const auto rk = mk(kc + kvb, 65536), rv = mk(vc + kvb, 65536);
    v4u K[8], V[8];
    const unsigned kvoff = ILV ? ww * 1024 + lane * 16 : (ww * 32 * 128 + lane * 8) * 2;
    constexpr int JS = ILV ? 8192 : 1024;
    __builtin_amdgcn_s_sleep(KSLEEP);
#pragma unroll
    for (int j = 0; j < 8; ++j) { if (j == WIN) __builtin_amdgcn_s_barrier(); if (j >= WIN) VMWAIT(WIN - 1); K[j] = __builtin_amdgcn_raw_buffer_load_b128(rk, kvoff, j * JS, 2); }
#pragma unroll
    for (int j = 0; j < 8; ++j) { VMWAIT(WIN - 1); V[j] = __builtin_amdgcn_raw_buffer_load_b128(rv, kvoff, j * JS, 2); if (j == KVI_AT) lds_arrive(LU + L_FLAG + F_KVI, lane); }
    SB();
    WSTAMP(1);
    lds_wait_ge<true>(FL + F_Q, 1);
    WSTAMP(2);
    const int c = lane & 15;
    unsigned pp;
    {
      const v4u q0 = *(const LAS v4u*)(LU + L_QB + 4 * c), q1 = *(const LAS v4u*)(LU + L_QB + 64 + 4 * c);
      float xs[16];
#pragma unroll
      for (int j = 0; j < 8; ++j) {
        float d0 = 0.f, d1 = 0.f;
#pragma unroll
        for (int k = 0; k < 4; ++k) { d0 = dot2(K[j][k], q0[k], d0); d1 = dot2(K[j][k], q1[k], d1); }
        xs[2 * j] = d0; xs[2 * j + 1] = d1;
      }
#define TR_STEP(HALF, CTRL, BIT) _Pragma("unroll") for (int h = 0; h < (HALF); ++h) { \
      const float lo_ = xs[h], hi_ = xs[h + (HALF)]; const float t1_ = lo_ + dpp<CTRL>(lo_), t2_ = hi_ + dpp<CTRL>(hi_); xs[h] = (c & (BIT)) ? t2_ : t1_; }
      TR_STEP(8, XOR8, 8) TR_STEP(4, XOR7, 4) TR_STEP(2, XOR2, 2) TR_STEP(1, XOR1, 1)
#undef TR_STEP
      const float s = rbf(rbf(xs[0]) * 0.08838834764831845f);
      float m = s;
      m = fmaxf(m, dpp<XOR2>(m)); m = fmaxf(m, dpp<ROR4>(m)); m = fmaxf(m, dpp<XOR8>(m)); m = max16(m); m = max32(m);
      if (lane < 2) L[L_MW + lane * 8 + ww] = m;
      lds_arrive(LU + L_FLAG + F_MCNT, lane);
      lds_wait_ge<false>(FL + F_MCNT, 8);
      WSTAMP(3);
      const v4f ma = *(const LAS v4f*)(L + L_MW + (lane & 1) * 8), mb = *(const LAS v4f*)(L + L_MW + (lane & 1) * 8 + 4);
      m = fmaxf(fmaxf(fmaxf(ma.x, ma.y), fmaxf(ma.z, ma.w)), fmaxf(fmaxf(mb.x, mb.y), fmaxf(mb.z, mb.w)));
      const float p = __builtin_amdgcn_exp2f((s - m) * LOG2E);
      float l = p;
      l += dpp<XOR2>(l); l += dpp<ROR4>(l); l += dpp<XOR8>(l); l = tr16(l, l); l = tr32(l, l);
      if (lane < 2) L[L_LW + lane * 8 + ww] = l;
      const unsigned pb = asu(rbf(p));
      pp = pkhi(pb, pb);
    }
    v4u msk;
#pragma unroll
    for (int k = 0; k < 4; ++k) msk[k] = ((c & 7) >> 1) == k ? ((c & 1) ? 0xffff0000u : 0x0000ffffu) : 0u;
    v4f acc = {0.f, 0.f, 0.f, 0.f};
#define PV_STEP(j) { \
      int pj = __builtin_amdgcn_update_dpp(0, (int)pp, 0x150 + 2 * j, 0xf, 0x3, false); \
      pj = __builtin_amdgcn_update_dpp(pj, (int)pp, 0x150 + 2 * j + 1, 0xf, 0xc, false); \
      const v4u af = {msk[0] & pj, msk[1] & pj, msk[2] & pj, msk[3] & pj}; \
      acc = __builtin_amdgcn_mfma_f32_16x16x32_bf16(__builtin_bit_cast(bf16x8, af), __builtin_bit_cast(bf16x8, V[j]), acc, 0, 0, 0); }
    PV_STEP(0) PV_STEP(1) PV_STEP(2) PV_STEP(3) PV_STEP(4) PV_STEP(5) PV_STEP(6) PV_STEP(7)
#undef PV_STEP
    *(LAS v4f*)(L + L_OP + ww * 256 + (lane >> 5) * 128 + 8 * c + 4 * ((lane >> 4) & 1)) = acc;
    lds_arrive(LU + L_FLAG + F_OCNT, lane);
    WSTAMP(4);
    WDUMP();
    return;
  }

  if (wave == 1) {
    const auto rq = mk(wq, 576 * 4096 * 2);
    const int qa = gi >> 2, qr = gi & 3;
    const v4u xa = __builtin_amdgcn_raw_buffer_load_b128(rx, lane * 16, 0, 0);
    const unsigned xb = __builtin_amdgcn_raw_buffer_load_b16(rx, 1024 + lane * 2, 0, 0);
    const unsigned e = __builtin_amdgcn_raw_buffer_load_b32(rws, WS_E, 0, 16);
    v4u WQ[18];
    const unsigned offq = ((144 * qr + 2 * (lane >> 3)) * 4096 + 256 * g + 64 * qa + 8 * (lane & 7)) * 2;
#pragma unroll
    for (int i = 0; i < 18; ++i) WQ[i] = __builtin_amdgcn_raw_buffer_load_b128(rq, offq, ((i >> 1) * 16 + (i & 1)) * 8192, 2);
    __builtin_amdgcn_s_barrier();
    SB();
    float v[9];
#pragma unroll
    for (int k = 0; k < 4; ++k) { v[2 * k] = lo16(xa[k]); v[2 * k + 1] = hi16(xa[k]); }
    v[8] = lo16(xb);
    layernorm9(v);
    store9(LU + L_XN, v, lane);
    WSTAMP(1);
    float a[8];
#pragma unroll
    for (int k = 0; k < 8; ++k) a[k] = 0.f;
#pragma unroll
    for (int i = 0; i < 9; ++i) {
      const unsigned xp = LU[L_XN + 72 * qr + 8 * i + (lane >> 3)];
#pragma unroll
      for (int c = 0; c < 4; ++c) {
        a[2 * c] = dot2(pklo(WQ[2 * i][c], WQ[2 * i + 1][c]), xp, a[2 * c]);
        a[2 * c + 1] = dot2(pkhi(WQ[2 * i][c], WQ[2 * i + 1][c]), xp, a[2 * c + 1]);
      }
    }
    const float r = red8(a, lane);
    const unsigned tg = __builtin_amdgcn_readfirstlane(e) + 1u;
    const int col = 8 * (lane & 7) + ((lane >> 3) & 1) * 4 + ((lane >> 4) & 1) * 2 + (lane >> 5);
    STSC8((v2u{asu(r), tg}), rws, WS_Q + (u * 64 + col) * 8);
    asm volatile("" ::: "memory");
    if (lane == 0) { FL[F_TAG] = tg; FL[F_QPUB] = 1u; }
    WSTAMP(2);
  } else {
#pragma unroll
    for (int k = 0; k < 5; ++k) { const int i = (k * 4 + (wave - 11)) * 64 + lane; if (i < 18 * 64) *(LAS v4u*)(LU + L_FR + 4 * i) = v4u{0u, 0u, 0u, 0u}; }
    LGKM0();
    __builtin_amdgcn_s_barrier();
  }
  {
    const int r = lane >> 4, c = lane & 15;
    const auto ro = mk(wo + (size_t)u * 16 * D, 16 * D * 2), r2 = mk(w2 + (size_t)u * 18 * D, 18 * D * 2);
    v4u W1[9], WF[9];
    const bool has1 = wave >= 11 && wave < 14;
    if (has1) {
      const auto r1 = mk(w1, 576 * F * 2);
      const int hw = wave - 11;
      const unsigned off1 = c < 12 ? ((36 * gi + r) * F + 288 * g + 96 * hw + 8 * c) * 2 : OOB;
      lds_wait_ge<true>(FL + F_KVI, W1_AT + hw);
      WSTAMP(3);
#pragma unroll
      for (int k = 0; k < 9; ++k) W1[k] = __builtin_amdgcn_raw_buffer_load_b128(r1, off1, k * (4 * F * 2), 2);
      lds_arrive(LU + L_FLAG + F_W1I, lane);
    }
    const int cb = wave == 1 ? 512 : 128 * (wave - 11);
    const unsigned off2 = (wave != 1 || c < 8) ? (r * D + cb + 8 * c) * 2 : OOB;
    lds_wait_ge<true>(FL + F_KVI, WF_AT);
    lds_wait_ge<true>(FL + F_W1I, 3);
    WSTAMP(4);
#pragma unroll
    for (int k = 0; k < 4; ++k) WF[k] = __builtin_amdgcn_raw_buffer_load_b128(ro, off2, k * (4 * D * 2), 2);
#pragma unroll
    for (int k = 0; k < 5; ++k) WF[4 + k] = LDNT(r2, off2 + k * (4 * D * 2));
    SB();
    lds_wait_ge<true>(FL + F_Y, 1);
    const unsigned tag = FL[F_TAG];
    const int cl = 8 * c + 4 * (r & 1) + 2 * (r >> 1);
    if (has1) {
      v4f acc = {0.f, 0.f, 0.f, 0.f};
#pragma unroll
      for (int k = 0; k < 9; ++k)
        acc = __builtin_amdgcn_mfma_f32_16x16x32_bf16(__builtin_bit_cast(bf16x8, *(const LAS v4u*)(LU + L_FY + k * 256 + 4 * lane)), __builtin_bit_cast(bf16x8, W1[k]), acc, 0, 0, 0);
      const auto s0 = __builtin_amdgcn_permlane32_swap(asu(acc[0]), asu(acc[2]), false, false);
      const auto s1 = __builtin_amdgcn_permlane32_swap(asu(acc[1]), asu(acc[3]), false, false);
      STSC((v4u{s0[0], tag, s1[0], tag}), rws, c < 12 ? WS_H + (u * 288 + 96 * (wave - 11) + cl) * 8 : OOB);
      lds_arrive(LU + L_FLAG + F_HPUB, lane);
      WSTAMP(5);
    }
    const unsigned poff = (wave != 1 || c < 8) ? WS_P + (u * 576 + cb + cl) * 8 : OOB;
    v4f acc = {0.f, 0.f, 0.f, 0.f}, acc2 = {0.f, 0.f, 0.f, 0.f};
    lds_wait_ge<false>(FL + F_CO, 1);
#pragma unroll
    for (int k = 0; k < 4; ++k)
      acc = __builtin_amdgcn_mfma_f32_16x16x32_bf16(__builtin_bit_cast(bf16x8, *(const LAS v4u*)(LU + L_FR + k * 256 + 4 * lane)), __builtin_bit_cast(bf16x8, WF[k]), acc, 0, 0, 0);
    asm volatile("" : "+v"(acc));
    lds_wait_ge<false>(FL + F_CH, 1);
#pragma unroll
    for (int k = 4; k < 9; ++k) {
      v4f& a = (k & 1) ? acc2 : acc;
      a = __builtin_amdgcn_mfma_f32_16x16x32_bf16(__builtin_bit_cast(bf16x8, *(const LAS v4u*)(LU + L_FR + k * 256 + 4 * lane)), __builtin_bit_cast(bf16x8, WF[k]), a, 0, 0, 0);
    }
    acc += acc2;
    const auto s0 = __builtin_amdgcn_permlane32_swap(asu(acc[0]), asu(acc[2]), false, false);
    const auto s1 = __builtin_amdgcn_permlane32_swap(asu(acc[1]), asu(acc[3]), false, false);
    STSC((v4u{s0[0], tag, s1[0], tag}), rws, poff);
    if (lane == 0) FL[F_PPUB] = 1u;
    WSTAMP(7);
  }
  WDUMP();
}

extern "C" void kernel_launch(void* const* d_in, const int* in_sizes, int n_in,
                              void* d_out, int out_size, void* d_ws, size_t ws_size,
                              hipStream_t stream) {
  (void)hipFuncSetAttribute((const void*)fused_decode, hipFuncAttributeMaxDynamicSharedMemorySize, LDS_BYTES);
  fused_decode<<<256, NTHR, LDS_BYTES, stream>>>((const u16*)d_in[0], (const u16*)d_in[1], (const u16*)d_in[2], (const u16*)d_in[3],
                                                 (const u16*)d_in[4], (const u16*)d_in[6], (const u16*)d_in[7], (const u16*)d_in[8],
                                                 (const u16*)d_in[9], (u16*)d_out, (unsigned char*)d_ws);
}
```

```cpp
#include <hip/hip_runtime.h>
#include <hip/hip_bf16.h>
#include <cstdint>

typedef unsigned short u16;
typedef unsigned v4u __attribute__((ext_vector_type(4)));
typedef unsigned v2u __attribute__((ext_vector_type(2)));
typedef float v4f __attribute__((ext_vector_type(4)));
typedef float v2f __attribute__((ext_vector_type(2)));
typedef __bf16 bf16x2 __attribute__((ext_vector_type(2)));
typedef short bf16x8 __attribute__((ext_vector_type(8)));
#define LAS __attribute__((address_space(3)))

#define WIN 4
#define WWIN 7
#define NOPOLL 0
#define ILV 1
#define KSLEEP 4
#define W1_AT 2
#define WF_AT 8
#define STAG 1
#define KVI_AT 7
#define CODE_KB 13
#define DLY_Q 30
#define DLY_O 16
#define DLY_H 16
#define DLY_P 16

constexpr int D = 576, SK = 65536, W = 4096, F = 4608;
constexpr int NTHR = 960;
constexpr unsigned OOB = 0x80000000u;
constexpr unsigned WS_E = 0;
constexpr unsigned WS_Q = 4096;
constexpr unsigned WS_H = WS_Q + 256 * 64 * 8;
constexpr unsigned WS_O = WS_H + 256 * 288 * 8;
constexpr unsigned WS_M = WS_O + 256 * 256 * 8;
constexpr unsigned WS_P = WS_M + 256 * 128;
constexpr unsigned WS_END = WS_P + 256 * 576 * 8;
constexpr unsigned WS_DBG = 8u << 20;
constexpr int L_XN = 0, L_Y = L_XN + 288, L_QB = L_Y + 288, L_MW = L_QB + 128, L_LW = L_MW + 16, L_CO = L_LW + 16,
              L_OP = L_CO + 32, L_FLAG = L_OP + 8 * 256, L_FR = L_FLAG + 32, L_FY = L_FR + 9 * 256, L_END = L_FY + 9 * 256;
constexpr int LDS_BYTES = 96 * 1024;
static_assert(L_END * 4 <= LDS_BYTES, "lds");
enum { F_CH = 11, F_W1I = 10, F_KVI = 9, F_Y = 0, F_QPUB, F_Q, F_MCNT, F_OCNT, F_HPUB, F_CO, F_PPUB, F_TAG };
constexpr unsigned SPIN_CAP = 1u << 22;
constexpr float LOG2E = 1.4426950408889634f;

__device__ __forceinline__ float rbf(float f) {
  unsigned u = __float_as_uint(f);
  u = (u + 0x7fffu + ((u >> 16) & 1u)) & 0xffff0000u;
  return __uint_as_float(u);
}
__device__ __forceinline__ float lo16(unsigned w) { return __uint_as_float(w << 16); }
__device__ __forceinline__ float hi16(unsigned w) { return __uint_as_float(w & 0xffff0000u); }
__device__ __forceinline__ float asf(unsigned w) { return __uint_as_float(w); }
__device__ __forceinline__ unsigned asu(float w) { return __float_as_uint(w); }
__device__ __forceinline__ unsigned pklo(unsigned a, unsigned b) { return __builtin_amdgcn_perm(b, a, 0x05040100u); }
__device__ __forceinline__ unsigned pkhi(unsigned a, unsigned b) { return __builtin_amdgcn_perm(b, a, 0x07060302u); }
__device__ __forceinline__ float dot2(unsigned k, unsigned q, float acc) {
  return __builtin_amdgcn_fdot2_f32_bf16(__builtin_bit_cast(bf16x2, k), __builtin_bit_cast(bf16x2, q), acc, false);
}
template <int CTRL> __device__ __forceinline__ float dpp(float x) {
  return __builtin_bit_cast(float, __builtin_amdgcn_mov_dpp(__builtin_bit_cast(int, x), CTRL, 0xf, 0xf, true));
}
template <int CTRL> __device__ __forceinline__ unsigned dppu(unsigned x) { return (unsigned)__builtin_amdgcn_mov_dpp((int)x, CTRL, 0xf, 0xf, true); }
constexpr int XOR1 = 0xB1, XOR2 = 0x4E, XOR3 = 0x1B, XOR7 = 0x141, XOR8 = 0x128, ROR4 = 0x124;
__device__ __forceinline__ float tr16(float a, float b) {
  auto s = __builtin_amdgcn_permlane16_swap(asu(a), asu(b), false, false);
  return asf(s[0]) + asf(s[1]);
}
__device__ __forceinline__ float tr32(float a, float b) {
  auto s = __builtin_amdgcn_permlane32_swap(asu(a), asu(b), false, false);
  return asf(s[0]) + asf(s[1]);
}
__device__ __forceinline__ float max16(float a) {
  auto s = __builtin_amdgcn_permlane16_swap(asu(a), asu(a), false, false);
  return fmaxf(asf(s[0]), asf(s[1]));
}
__device__ __forceinline__ float max32(float a) {
  auto s = __builtin_amdgcn_permlane32_swap(asu(a), asu(a), false, false);
  return fmaxf(asf(s[0]), asf(s[1]));
}
__device__ __forceinline__ float wave_sum(float t) {
  t += dpp<XOR1>(t); t += dpp<XOR2>(t); t += dpp<ROR4>(t); t += dpp<XOR8>(t);
  t = tr16(t, t); t = tr32(t, t);
  return t;
}
__device__ __forceinline__ float red8(float (&a)[8], int lane) {
#pragma unroll
  for (int h = 0; h < 4; ++h) { const float t1 = a[h] + dpp<XOR8>(a[h]), t2 = a[h + 4] + dpp<XOR8>(a[h + 4]); a[h] = (lane & 8) ? t2 : t1; }
  a[0] = tr16(a[0], a[2]); a[1] = tr16(a[1], a[3]);
  return tr32(a[0], a[1]);
}
#define VMWAIT(n) asm volatile("s_waitcnt vmcnt(%0)" :: "n"(n) : "memory")
#define LGKM0() asm volatile("s_waitcnt lgkmcnt(0)" ::: "memory")
#define LDNT(r, off) __builtin_amdgcn_raw_buffer_load_b128((r), (off), 0, 2)
#define LDNT16(r, off) __builtin_amdgcn_raw_buffer_load_b16((r), (off), 0, 2)
#define LDSC(r, off, so) __builtin_amdgcn_raw_buffer_load_b128((r), (off), (so), 16)
#define LDSC8(r, off, so) __builtin_amdgcn_raw_buffer_load_b64((r), (off), (so), 16)
#define STSC(v, r, off) __builtin_amdgcn_raw_buffer_store_b128((v), (r), (off), 0, 16)
#define STSC8(v, r, off) __builtin_amdgcn_raw_buffer_store_b64((v), (r), (off), 0, 16)
#define SB() __builtin_amdgcn_sched_barrier(0)
#define PIN8(a) asm volatile("" : "+v"(a[0]), "+v"(a[1]), "+v"(a[2]), "+v"(a[3]), "+v"(a[4]), "+v"(a[5]), "+v"(a[6]), "+v"(a[7]))

template <bool SLEEP> __device__ __forceinline__ void lds_wait_ge(volatile LAS unsigned* p, unsigned v) {
#pragma unroll 1
  for (unsigned n = 0; n < SPIN_CAP; ++n) { if (*p >= v) break; if (SLEEP) __builtin_amdgcn_s_sleep(1); }
  asm volatile("" ::: "memory");
}
__device__ __forceinline__ void lds_arrive(LAS unsigned* p, int lane) {
  asm volatile("" ::: "memory");
  if (lane == 0) (void)__hip_atomic_fetch_add(p, 1u, __ATOMIC_RELAXED, __HIP_MEMORY_SCOPE_WORKGROUP);
  asm volatile("" ::: "memory");
}

__device__ __forceinline__ void layernorm9(float (&v)[9]) {
  float s = ((v[0] + v[1]) + (v[2] + v[3])) + ((v[4] + v[5]) + (v[6] + v[7])) + v[8];
  const float mean = wave_sum(s) * (1.f / D);
  float s2 = 0.f, s3 = 0.f;
#pragma unroll
  for (int k = 0; k < 8; k += 2) { const float d = v[k] - mean, e = v[k + 1] - mean; s2 = fmaf(d, d, s2); s3 = fmaf(e, e, s3); }
  { const float d = v[8] - mean; s2 = fmaf(d, d, s2); }
  const float var = rbf(wave_sum(s2 + s3) * (1.f / D));
  const float mb = rbf(mean);
  const float den = rbf(__builtin_amdgcn_sqrtf(rbf(var + 1e-5f)));
  const float inv = __builtin_amdgcn_rcpf(den);
#pragma unroll
  for (int k = 0; k < 9; ++k) {
    const float n = rbf(v[k] - mb);
    float qv = n * inv;
    qv = fmaf(fmaf(-qv, den, n), inv, qv);
    v[k] = rbf(qv);
  }
}
__device__ __forceinline__ void store9(LAS unsigned* dst, const float (&v)[9], int lane) {
  *(LAS v4u*)(dst + 4 * lane) = v4u{pkhi(asu(v[0]), asu(v[1])), pkhi(asu(v[2]), asu(v[3])), pkhi(asu(v[4]), asu(v[5])), pkhi(asu(v[6]), asu(v[7]))};
  ((LAS u16*)dst)[512 + lane] = (u16)(asu(v[8]) >> 16);
}

#define WSTAMP(k) do {} while (0)
#define WDUMP() do {} while (0)

__global__ void __launch_bounds__(NTHR) fused_decode(const u16* __restrict__ x, const u16* __restrict__ kc, const u16* __restrict__ vc,
                                                     const u16* __restrict__ wq, const u16* __restrict__ wo, const u16* __restrict__ w1,
                                                     const u16* __restrict__ b1, const u16* __restrict__ w2, const u16* __restrict__ b2,
                                                     u16* __restrict__ out, unsigned char* ws) {
  const unsigned long long T0 = __builtin_amdgcn_s_memrealtime();
  unsigned long long pc0;
  asm volatile("s_getpc_b64 %0" : "=s"(pc0));
  extern __shared__ __attribute__((aligned(16))) float smem[];
  LAS float* L = (LAS float*)smem;
  LAS unsigned* LU = (LAS unsigned*)smem;
  volatile LAS unsigned* FL = (volatile LAS unsigned*)(LU + L_FLAG);
  const int t = threadIdx.x, lane = t & 63;
  const int wave = __builtin_amdgcn_readfirstlane(t >> 6);
  const int u = blockIdx.x, g = u >> 4, gi = u & 15;
  auto mk = [](const void* p, unsigned bytes) { return __builtin_amdgcn_make_buffer_rsrc((void*)p, 0, bytes, 0x00020000); };
  const auto rws = mk(ws, WS_END);
  const auto rx = mk(x, D * 2);

  if (wave == 0) {
    const int xcd = u & 7, jj = u >> 3;
    const int lg = (jj >> 2) * 8 + xcd;
    const bool is_red = lg < 36;
    const int rcol = 16 * lg + 4 * (jj & 3);
    const auto rc = mk((const void*)(pc0 & ~255ull), CODE_KB * 1024);
    const v4u cpf = __builtin_amdgcn_raw_buffer_load_b128(rc, (jj % CODE_KB) * 1024 + lane * 16, 0, 0);
    const auto rb2 = mk(b2, D * 2);
    const int oc = 2 * (lane & 1) + (lane >> 5);
    const unsigned xrr = __builtin_amdgcn_raw_buffer_load_b16(rx, is_red ? (rcol + oc) * 2 : OOB, 0, 0);
    const unsigned b2r = __builtin_amdgcn_raw_buffer_load_b16(rb2, is_red ? (rcol + oc) * 2 : OOB, 0, 0);
    if (lane < 32) LU[L_FLAG + lane] = 0u;
    LGKM0();
    __builtin_amdgcn_s_barrier();
    SB();
    asm volatile("" :: "v"(cpf));
    lds_wait_ge<true>(FL + F_QPUB, 1);
    const unsigned tag = __builtin_amdgcn_readfirstlane(FL[F_TAG]);
    __builtin_amdgcn_s_sleep(DLY_Q);
    WSTAMP(1);
    {
      v4u qv[8];
      const unsigned qoff = WS_Q + g * 8192 + lane * 16;
#pragma unroll 1
      for (unsigned n = 0; n < SPIN_CAP; ++n) {
        asm volatile("" ::: "memory");
        unsigned bad = 0;
#pragma unroll
        for (int k = 0; k < 8; ++k) qv[k] = LDSC(rws, (NOPOLL & 1) ? OOB : qoff, k * 1024);
#pragma unroll
        for (int k = 0; k < 8; ++k) bad |= (qv[k].y ^ tag) | (qv[k].w ^ tag);
        if (__all(bad == 0) || (NOPOLL & 1)) break;
      }
      float s[8];
#pragma unroll
      for (int a = 0; a < 4; ++a) { s[2 * a] = asf(qv[2 * a].x) + asf(qv[2 * a + 1].x); s[2 * a + 1] = asf(qv[2 * a].z) + asf(qv[2 * a + 1].z); }
      const float r0 = tr32(s[0], s[4]), r1_ = tr32(s[1], s[5]), r2_ = tr32(s[2], s[6]), r3 = tr32(s[3], s[7]);
      const int a0 = (lane >> 5) * 2;
      LU[L_QB + a0 * 32 + (lane & 31)] = pkhi(asu(rbf(r0)), asu(rbf(r1_)));
      LU[L_QB + (a0 + 1) * 32 + (lane & 31)] = pkhi(asu(rbf(r2_)), asu(rbf(r3)));
      asm volatile("" ::: "memory");
      if (lane == 0) FL[F_Q] = 1u;
      WSTAMP(2);
    }
    lds_wait_ge<false>(FL + F_OCNT, 8);
    WSTAMP(3);
    {
      v2f oa = *(const LAS v2f*)(L + L_OP + 2 * lane), ob = *(const LAS v2f*)(L + L_OP + 128 + 2 * lane);
#pragma unroll
      for (int w = 1; w < 8; ++w) { oa += *(const LAS v2f*)(L + L_OP + w * 256 + 2 * lane); ob += *(const LAS v2f*)(L + L_OP + w * 256 + 128 + 2 * lane); }
      const int hd = lane & 1;
      const v4f la = *(const LAS v4f*)(L + L_LW + hd * 8), lb = *(const LAS v4f*)(L + L_LW + hd * 8 + 4);
      const v4f ma = *(const LAS v4f*)(L + L_MW + hd * 8), mb = *(const LAS v4f*)(L + L_MW + hd * 8 + 4);
      const float Ls = ((la.x + la.y) + (la.z + la.w)) + ((lb.x + lb.y) + (lb.z + lb.w));
      const float M = fmaxf(fmaxf(fmaxf(ma.x, ma.y), fmaxf(ma.z, ma.w)), fmaxf(fmaxf(mb.x, mb.y), fmaxf(mb.z, mb.w)));
      STSC((v4u{asu(oa.x), tag, asu(oa.y), tag}), rws, WS_O + u * 2048 + lane * 16);
      STSC((v4u{asu(ob.x), tag, asu(ob.y), tag}), rws, WS_O + u * 2048 + 1024 + lane * 16);
      STSC((v4u{asu(M), tag, asu(Ls), tag}), rws, lane < 8 ? WS_M + u * 128 + lane * 16 : OOB);
    }
    WSTAMP(4);
    __builtin_amdgcn_s_sleep(DLY_O);
    {
      const unsigned ooff = WS_O + (16 * g + (lane >> 4)) * 2048 + (16 * gi + (lane & 15)) * 8;
      const unsigned moff = WS_M + (16 * g + (lane >> 4)) * 128 + (gi >> 3) * 16;
      v2u ov[4]; v4u ml[4];
#pragma unroll 1
      for (unsigned n = 0; n < SPIN_CAP; ++n) {
        asm volatile("" ::: "memory");
        unsigned bad = 0;
#pragma unroll
        for (int k = 0; k < 4; ++k) { ov[k] = LDSC8(rws, (NOPOLL & 2) ? OOB : ooff, k * 8192); ml[k] = LDSC(rws, (NOPOLL & 2) ? OOB : moff, k * 512); }
#pragma unroll
        for (int k = 0; k < 4; ++k) bad |= (ov[k].y ^ tag) | (ml[k].y ^ tag) | (ml[k].w ^ tag);
        if (__all(bad == 0) || (NOPOLL & 2)) break;
      }
      WSTAMP(5);
      float M = fmaxf(fmaxf(asf(ml[0].x), asf(ml[1].x)), fmaxf(asf(ml[2].x), asf(ml[3].x)));
      M = max16(M); M = max32(M);
      float num = 0.f, den = 0.f;
#pragma unroll
      for (int k = 0; k < 4; ++k) { const float sc = __builtin_amdgcn_exp2f((asf(ml[k].x) - M) * LOG2E); num = fmaf(sc, asf(ov[k].x), num); den = fmaf(sc, asf(ml[k].z), den); }
      num = tr16(num, num); den = tr16(den, den); num = tr32(num, num); den = tr32(den, den);
      const float o = rbf(num * __builtin_amdgcn_rcpf(den));
      {
        const int d = lane & 15, m0 = 2 * (lane >> 4);
        LAS u16* fp = (LAS u16*)(LU + L_FR) + ((d >> 2) * 64 + (d & 3) * 16 + m0) * 8 + m0;
        fp[0] = (u16)(asu(o) >> 16); fp[9] = (u16)(asu(o) >> 16);
      }
      asm volatile("" ::: "memory");
      if (lane == 0) FL[F_CO] = 1u;
      WSTAMP(6);
    }
    if (is_red) {
      const unsigned off = WS_P + ((lane >> 1) * 576 + rcol + 2 * (lane & 1)) * 8;
      const float xb2 = lo16(xrr) + lo16(b2r);
      lds_wait_ge<false>(FL + F_PPUB, 1);
      __builtin_amdgcn_s_sleep(DLY_P);
      v4u pv[8];
#pragma unroll 1
      for (unsigned n = 0; n < SPIN_CAP; ++n) {
        asm volatile("" ::: "memory");
        unsigned bad = 0;
#pragma unroll
        for (int k = 0; k < 8; ++k) pv[k] = LDSC(rws, (NOPOLL & 4) ? OOB : off, k * (32 * 576 * 8));
#pragma unroll
        for (int k = 0; k < 8; ++k) bad |= (pv[k].y ^ tag) | (pv[k].w ^ tag);
        if (__all(bad == 0) || (NOPOLL & 4)) break;
      }
      float s0 = ((asf(pv[0].x) + asf(pv[1].x)) + (asf(pv[2].x) + asf(pv[3].x))) + ((asf(pv[4].x) + asf(pv[5].x)) + (asf(pv[6].x) + asf(pv[7].x)));
      float s1 = ((asf(pv[0].z) + asf(pv[1].z)) + (asf(pv[2].z) + asf(pv[3].z))) + ((asf(pv[4].z) + asf(pv[5].z)) + (asf(pv[6].z) + asf(pv[7].z)));
      s0 = tr32(s0, s1);
      s0 += dpp<XOR2>(s0); s0 += dpp<ROR4>(s0); s0 += dpp<XOR8>(s0); s0 = tr16(s0, s0);
      if ((lane & 30) == 0) out[rcol + oc] = (u16)(asu(rbf(s0 + xb2)) >> 16);
      if (u == 0 && lane == 0) __builtin_amdgcn_raw_buffer_store_b32(tag, rws, WS_E, 0, 16);
      if (u == 0) { unsigned long long d = 0;
        if ((unsigned long long)ws & 4095) d += 2000; if ((unsigned long long)kc & 4095) d += 4000; if ((unsigned long long)wq & 4095) d += 8000;
        if (((unsigned long long)ws | (unsigned long long)kc | (unsigned long long)vc | (unsigned long long)wq | (unsigned long long)wo | (unsigned long long)w1 | (unsigned long long)w2) & 127) d += 16000;
        if (((unsigned long long)kc | (unsigned long long)ws) & ((2u << 20) - 1)) d += 32000;
        const unsigned long long T2 = __builtin_amdgcn_s_memrealtime() + d; while (__builtin_amdgcn_s_memrealtime() < T2) __builtin_amdgcn_s_sleep(1); }
      WSTAMP(7);
    }
    WDUMP();
    return;
  }
  if (wave == 10) {
    const auto rb1 = mk(b1, F * 2);
    const unsigned b1r = LDNT16(rb1, (lane & 31) < 18 ? (18 * u + (lane & 31)) * 2 : OOB);
    __builtin_amdgcn_s_barrier();
    lds_wait_ge<true>(FL + F_QPUB, 1);
    const unsigned tag = __builtin_amdgcn_readfirstlane(FL[F_TAG]);
    {
      const v4u xa = *(const LAS v4u*)(LU + L_XN + 4 * lane);
      const unsigned xb = ((const LAS u16*)(LU + L_XN))[512 + lane];
      float v[9];
#pragma unroll
      for (int k = 0; k < 4; ++k) { v[2 * k] = lo16(xa[k]); v[2 * k + 1] = hi16(xa[k]); }
      v[8] = lo16(xb);
      layernorm9(v);
      store9(LU + L_Y, v, lane);
#pragma unroll
      for (int it = 0; it < 5; ++it) {
        const int idx = it * 64 + lane, n = idx < 288 ? idx >> 3 : 35, m = idx & 7;
        ((LAS u16*)(LU + L_FY))[((n >> 2) * 64 + (n & 3) * 16 + m) * 8 + m] = ((const LAS u16*)(LU + L_Y))[36 * gi + n];
      }
    }
    asm volatile("" ::: "memory");
    if (lane == 0) FL[F_Y] = 1u;
    WSTAMP(1);
    const unsigned hoff = (lane & 31) < 18 ? WS_H + ((16 * g + (lane >> 5)) * 288 + 18 * gi + (lane & 31)) * 8 : OOB;
    const unsigned tagx = (lane & 31) < 18 ? tag : 0u;
    lds_wait_ge<true>(FL + F_HPUB, 3);
    __builtin_amdgcn_s_sleep(DLY_H);
    WSTAMP(2);
    v2u hv[8];
#pragma unroll 1
    for (unsigned n = 0; n < SPIN_CAP; ++n) {
      asm volatile("" ::: "memory");
      unsigned bad = 0;
#pragma unroll
      for (int k = 0; k < 8; ++k) hv[k] = LDSC8(rws, (NOPOLL & 2) ? OOB : hoff, k * (2 * 288 * 8));
#pragma unroll
      for (int k = 0; k < 8; ++k) bad |= hv[k].y ^ tagx;
      if (__all(bad == 0) || (NOPOLL & 2)) break;
    }
    WSTAMP(3);
    float pre = ((asf(hv[0].x) + asf(hv[1].x)) + (asf(hv[2].x) + asf(hv[3].x))) + ((asf(hv[4].x) + asf(hv[5].x)) + (asf(hv[6].x) + asf(hv[7].x)));
    pre = tr32(pre, pre);
    const float tt = rbf(rbf(pre) + lo16(b1r));
    const float inner = 0.7978845608028654f * (tt + 0.044715f * tt * tt * tt);
    const float th = 1.f - 2.f * __builtin_amdgcn_rcpf(__builtin_amdgcn_exp2f(inner * (2.f * LOG2E)) + 1.f);
    const float h = rbf(0.5f * tt * (1.f + th));
    if ((lane & 31) < 18) {
      const int n = 16 + (lane & 31), m0 = 4 * (lane >> 5);
      LAS u16* fp = (LAS u16*)(LU + L_FR) + ((n >> 2) * 64 + (n & 3) * 16 + m0) * 8 + m0;
      fp[0] = (u16)(asu(h) >> 16); fp[9] = (u16)(asu(h) >> 16); fp[18] = (u16)(asu(h) >> 16); fp[27] = (u16)(asu(h) >> 16);
    }
    asm volatile("" ::: "memory");
    if (lane == 0) FL[F_CH] = 1u;
    WSTAMP(4);
    WDUMP();
    return;
  }

  if (wave >= 2 && wave < 10) {
    const int ww = wave - 2;
    const size_t kvb = ((size_t)g * SK + (SK - W) + gi * 256) * 128;
    const auto rk = mk(kc + kvb, 65536), rv = mk(vc + kvb, 65536);
    v4u K[8], V[8];
    const unsigned kvoff = ILV ? ww * 1024 + lane * 16 : (ww * 32 * 128 + lane * 8) * 2;
    constexpr int JS = ILV ? 8192 : 1024;
    __builtin_amdgcn_s_sleep(KSLEEP);
#pragma unroll
    for (int j = 0; j < 8; ++j) { if (j == WIN) __builtin_amdgcn_s_barrier(); if (j >= WIN) VMWAIT(WIN - 1); K[j] = __builtin_amdgcn_raw_buffer_load_b128(rk, kvoff, j * JS, 2); }
#pragma unroll
    for (int j = 0; j < 8; ++j) { VMWAIT(WIN - 1); V[j] = __builtin_amdgcn_raw_buffer_load_b128(rv, kvoff, j * JS, 2); if (j == KVI_AT) lds_arrive(LU + L_FLAG + F_KVI, lane); }
    SB();
    WSTAMP(1);
    lds_wait_ge<true>(FL + F_Q, 1);
    WSTAMP(2);
    const int c = lane & 15;
    unsigned pp;
    {
      const v4u q0 = *(const LAS v4u*)(LU + L_QB + 4 * c), q1 = *(const LAS v4u*)(LU + L_QB + 64 + 4 * c);
      float xs[16];
#pragma unroll
      for (int j = 0; j < 8; ++j) {
        float d0 = 0.f, d1 = 0.f;
#pragma unroll
        for (int k = 0; k < 4; ++k) { d0 = dot2(K[j][k], q0[k], d0); d1 = dot2(K[j][k], q1[k], d1); }
        xs[2 * j] = d0; xs[2 * j + 1] = d1;
      }
#define TR_STEP(HALF, CTRL, BIT) _Pragma("unroll") for (int h = 0; h < (HALF); ++h) { \
      const float lo_ = xs[h], hi_ = xs[h + (HALF)]; const float t1_ = lo_ + dpp<CTRL>(lo_), t2_ = hi_ + dpp<CTRL>(hi_); xs[h] = (c & (BIT)) ? t2_ : t1_; }
      TR_STEP(8, XOR8, 8) TR_STEP(4, XOR7, 4) TR_STEP(2, XOR2, 2) TR_STEP(1, XOR1, 1)
#undef TR_STEP
      const float s = rbf(rbf(xs[0]) * 0.08838834764831845f);
      float m = s;
      m = fmaxf(m, dpp<XOR2>(m)); m = fmaxf(m, dpp<ROR4>(m)); m = fmaxf(m, dpp<XOR8>(m)); m = max16(m); m = max32(m);
      if (lane < 2) L[L_MW + lane * 8 + ww] = m;
      lds_arrive(LU + L_FLAG + F_MCNT, lane);
      lds_wait_ge<false>(FL + F_MCNT, 8);
      WSTAMP(3);
      const v4f ma = *(const LAS v4f*)(L + L_MW + (lane & 1) * 8), mb = *(const LAS v4f*)(L + L_MW + (lane & 1) * 8 + 4);
      m = fmaxf(fmaxf(fmaxf(ma.x, ma.y), fmaxf(ma.z, ma.w)), fmaxf(fmaxf(mb.x, mb.y), fmaxf(mb.z, mb.w)));
      const float p = __builtin_amdgcn_exp2f((s - m) * LOG2E);
      float l = p;
      l += dpp<XOR2>(l); l += dpp<ROR4>(l); l += dpp<XOR8>(l); l = tr16(l, l); l = tr32(l, l);
      if (lane < 2) L[L_LW + lane * 8 + ww] = l;
      const unsigned pb = asu(rbf(p));
      pp = pkhi(pb, pb);
    }
    v4u msk;
#pragma unroll
    for (int k = 0; k < 4; ++k) msk[k] = ((c & 7) >> 1) == k ? ((c & 1) ? 0xffff0000u : 0x0000ffffu) : 0u;
    v4f acc = {0.f, 0.f, 0.f, 0.f};
#define PV_STEP(j) { \
      int pj = __builtin_amdgcn_update_dpp(0, (int)pp, 0x150 + 2 * j, 0xf, 0x3, false); \
      pj = __builtin_amdgcn_update_dpp(pj, (int)pp, 0x150 + 2 * j + 1, 0xf, 0xc, false); \
      const v4u af = {msk[0] & pj, msk[1] & pj, msk[2] & pj, msk[3] & pj}; \
      acc = __builtin_amdgcn_mfma_f32_16x16x32_bf16(__builtin_bit_cast(bf16x8, af), __builtin_bit_cast(bf16x8, V[j]), acc, 0, 0, 0); }
    PV_STEP(0) PV_STEP(1) PV_STEP(2) PV_STEP(3) PV_STEP(4) PV_STEP(5) PV_STEP(6) PV_STEP(7)
#undef PV_STEP
    *(LAS v4f*)(L + L_OP + ww * 256 + (lane >> 5) * 128 + 8 * c + 4 * ((lane >> 4) & 1)) = acc;
    lds_arrive(LU + L_FLAG + F_OCNT, lane);
    WSTAMP(4);
    WDUMP();
    return;
  }

  if (wave == 1) {
    const auto rq = mk(wq, 576 * 4096 * 2);
    const int qa = gi >> 2, qr = gi & 3;
    const v4u xa = __builtin_amdgcn_raw_buffer_load_b128(rx, lane * 16, 0, 0);
    const unsigned xb = __builtin_amdgcn_raw_buffer_load_b16(rx, 1024 + lane * 2, 0, 0);
    const unsigned e = __builtin_amdgcn_raw_buffer_load_b32(rws, WS_E, 0, 16);
    v4u WQ[18];
    const unsigned offq = ((144 * qr + 2 * (lane >> 3)) * 4096 + 256 * g + 64 * qa + 8 * (lane & 7)) * 2;
#pragma unroll
    for (int i = 0; i < 18; ++i) WQ[i] = __builtin_amdgcn_raw_buffer_load_b128(rq, offq, ((i >> 1) * 16 + (i & 1)) * 8192, 2);
    __builtin_amdgcn_s_barrier();
    SB();
    float v[9];
#pragma unroll
    for (int k = 0; k < 4; ++k) { v[2 * k] = lo16(xa[k]); v[2 * k + 1] = hi16(xa[k]); }
    v[8] = lo16(xb);
    layernorm9(v);
    store9(LU + L_XN, v, lane);
    WSTAMP(1);
    float a[8];
#pragma unroll
    for (int k = 0; k < 8; ++k) a[k] = 0.f;
#pragma unroll
    for (int i = 0; i < 9; ++i) {
      const unsigned xp = LU[L_XN + 72 * qr + 8 * i + (lane >> 3)];
#pragma unroll
      for (int c = 0; c < 4; ++c) {
        a[2 * c] = dot2(pklo(WQ[2 * i][c], WQ[2 * i + 1][c]), xp, a[2 * c]);
        a[2 * c + 1] = dot2(pkhi(WQ[2 * i][c], WQ[2 * i + 1][c]), xp, a[2 * c + 1]);
      }
    }
    const float r = red8(a, lane);
    const unsigned tg = __builtin_amdgcn_readfirstlane(e) + 1u;
    const int col = 8 * (lane & 7) + ((lane >> 3) & 1) * 4 + ((lane >> 4) & 1) * 2 + (lane >> 5);
    STSC8((v2u{asu(r), tg}), rws, WS_Q + (u * 64 + col) * 8);
    asm volatile("" ::: "memory");
    if (lane == 0) { FL[F_TAG] = tg; FL[F_QPUB] = 1u; }
    WSTAMP(2);
  } else {
#pragma unroll
    for (int k = 0; k < 5; ++k) { const int i = (k * 4 + (wave - 11)) * 64 + lane; if (i < 18 * 64) *(LAS v4u*)(LU + L_FR + 4 * i) = v4u{0u, 0u, 0u, 0u}; }
    LGKM0();
    __builtin_amdgcn_s_barrier();
  }
  {
    const int r = lane >> 4, c = lane & 15;
    const auto ro = mk(wo + (size_t)u * 16 * D, 16 * D * 2), r2 = mk(w2 + (size_t)u * 18 * D, 18 * D * 2);
    v4u W1[9], WF[9];
    const bool has1 = wave >= 11 && wave < 14;
    if (has1) {
      const auto r1 = mk(w1, 576 * F * 2);
      const int hw = wave - 11;
      const unsigned off1 = c < 12 ? ((36 * gi + r) * F + 288 * g + 96 * hw + 8 * c) * 2 : OOB;
      lds_wait_ge<true>(FL + F_KVI, W1_AT + hw);
      WSTAMP(3);
#pragma unroll
      for (int k = 0; k < 9; ++k) W1[k] = __builtin_amdgcn_raw_buffer_load_b128(r1, off1, k * (4 * F * 2), 2);
      lds_arrive(LU + L_FLAG + F_W1I, lane);
    }
    const int cb = wave == 1 ? 512 : 128 * (wave - 11);
    const unsigned off2 = (wave != 1 || c < 8) ? (r * D + cb + 8 * c) * 2 : OOB;
    lds_wait_ge<true>(FL + F_KVI, WF_AT);
    lds_wait_ge<true>(FL + F_W1I, 3);
    WSTAMP(4);
#pragma unroll
    for (int k = 0; k < 4; ++k) WF[k] = __builtin_amdgcn_raw_buffer_load_b128(ro, off2, k * (4 * D * 2), 2);
#pragma unroll
    for (int k = 0; k < 5; ++k) WF[4 + k] = LDNT(r2, off2 + k * (4 * D * 2));
    SB();
    lds_wait_ge<true>(FL + F_Y, 1);
    const unsigned tag = FL[F_TAG];
    const int cl = 8 * c + 4 * (r & 1) + 2 * (r >> 1);
    if (has1) {
      v4f acc = {0.f, 0.f, 0.f, 0.f};
#pragma unroll
      for (int k = 0; k < 9; ++k)
        acc = __builtin_amdgcn_mfma_f32_16x16x32_bf16(__builtin_bit_cast(bf16x8, *(const LAS v4u*)(LU + L_FY + k * 256 + 4 * lane)), __builtin_bit_cast(bf16x8, W1[k]), acc, 0, 0, 0);
      const auto s0 = __builtin_amdgcn_permlane32_swap(asu(acc[0]), asu(acc[2]), false, false);
      const auto s1 = __builtin_amdgcn_permlane32_swap(asu(acc[1]), asu(acc[3]), false, false);
      STSC((v4u{s0[0], tag, s1[0], tag}), rws, c < 12 ? WS_H + (u * 288 + 96 * (wave - 11) + cl) * 8 : OOB);
      lds_arrive(LU + L_FLAG + F_HPUB, lane);
      WSTAMP(5);
    }
    const unsigned poff = (wave != 1 || c < 8) ? WS_P + (u * 576 + cb + cl) * 8 : OOB;
    v4f acc = {0.f, 0.f, 0.f, 0.f}, acc2 = {0.f, 0.f, 0.f, 0.f};
    lds_wait_ge<false>(FL + F_CO, 1);
#pragma unroll
    for (int k = 0; k < 4; ++k)
      acc = __builtin_amdgcn_mfma_f32_16x16x32_bf16(__builtin_bit_cast(bf16x8, *(const LAS v4u*)(LU + L_FR + k * 256 + 4 * lane)), __builtin_bit_cast(bf16x8, WF[k]), acc, 0, 0, 0);
    asm volatile("" : "+v"(acc));
    lds_wait_ge<false>(FL + F_CH, 1);
#pragma unroll
    for (int k = 4; k < 9; ++k) {
      v4f& a = (k & 1) ? acc2 : acc;
      a = __builtin_amdgcn_mfma_f32_16x16x32_bf16(__builtin_bit_cast(bf16x8, *(const LAS v4u*)(LU + L_FR + k * 256 + 4 * lane)), __builtin_bit_cast(bf16x8, WF[k]), a, 0, 0, 0);
    }
    acc += acc2;
    const auto s0 = __builtin_amdgcn_permlane32_swap(asu(acc[0]), asu(acc[2]), false, false);
    const auto s1 = __builtin_amdgcn_permlane32_swap(asu(acc[1]), asu(acc[3]), false, false);
    STSC((v4u{s0[0], tag, s1[0], tag}), rws, poff);
    if (lane == 0) FL[F_PPUB] = 1u;
    WSTAMP(7);
  }
  WDUMP();
}

extern "C" void kernel_launch(void* const* d_in, const int* in_sizes, int n_in,
                              void* d_out, int out_size, void* d_ws, size_t ws_size,
                              hipStream_t stream) {
  (void)hipFuncSetAttribute((const void*)fused_decode, hipFuncAttributeMaxDynamicSharedMemorySize, LDS_BYTES);
  fused_decode<<<256, NTHR, LDS_BYTES, stream>>>((const u16*)d_in[0], (const u16*)d_in[1], (const u16*)d_in[2], (const u16*)d_in[3],
                                                 (const u16*)d_in[4], (const u16*)d_in[6], (const u16*)d_in[7], (const u16*)d_in[8],
                                                 (const u16*)d_in[9], (u16*)d_out, (unsigned char*)d_ws);
}
```

```cpp
#include <hip/hip_runtime.h>
#include <hip/hip_bf16.h>
#include <cstdint>

typedef unsigned short u16;
typedef unsigned v4u __attribute__((ext_vector_type(4)));
typedef unsigned v2u __attribute__((ext_vector_type(2)));
typedef float v4f __attribute__((ext_vector_type(4)));
typedef float v2f __attribute__((ext_vector_type(2)));
typedef __bf16 bf16x2 __attribute__((ext_vector_type(2)));
typedef short bf16x8 __attribute__((ext_vector_type(8)));
#define LAS __attribute__((address_space(3)))

#define WIN 4
#define WWIN 7
#define NOPOLL 0
#define ILV 1
#define KSLEEP 4
#define W1_AT 2
#define WF_AT 8
#define STAG 1
#define KVI_AT 7
#define CODE_KB 13
#define DLY_Q 30
#define DLY_O 16
#define DLY_H 16
#define DLY_P 16

constexpr int D = 576, SK = 65536, W = 4096, F = 4608;
constexpr int NTHR = 960;
constexpr unsigned OOB = 0x80000000u;
constexpr unsigned WS_E = 0;
constexpr unsigned WS_Q = 4096;
constexpr unsigned WS_H = WS_Q + 256 * 64 * 8;
constexpr unsigned WS_O = WS_H + 256 * 288 * 8;
constexpr unsigned WS_M = WS_O + 256 * 256 * 8;
constexpr unsigned WS_P = WS_M + 256 * 128;
constexpr unsigned WS_END = WS_P + 256 * 576 * 8;
constexpr unsigned WS_DBG = 8u << 20;
constexpr int L_XN = 0, L_Y = L_XN + 288, L_QB = L_Y + 288, L_MW = L_QB + 128, L_LW = L_MW + 16, L_CO = L_LW + 16,
              L_OP = L_CO + 32, L_FLAG = L_OP + 8 * 256, L_FR = L_FLAG + 32, L_FY = L_FR + 9 * 256, L_END = L_FY + 9 * 256;
constexpr int LDS_BYTES = 96 * 1024;
static_assert(L_END * 4 <= LDS_BYTES, "lds");
enum { F_CH = 11, F_W1I = 10, F_KVI = 9, F_Y = 0, F_QPUB, F_Q, F_MCNT, F_OCNT, F_HPUB, F_CO, F_PPUB, F_TAG };
constexpr unsigned SPIN_CAP = 1u << 22;
constexpr float LOG2E = 1.4426950408889634f;

__device__ __forceinline__ float rbf(float f) {
  unsigned u = __float_as_uint(f);
  u = (u + 0x7fffu + ((u >> 16) & 1u)) & 0xffff0000u;
  return __uint_as_float(u);
}
__device__ __forceinline__ float lo16(unsigned w) { return __uint_as_float(w << 16); }
__device__ __forceinline__ float hi16(unsigned w) { return __uint_as_float(w & 0xffff0000u); }
__device__ __forceinline__ float asf(unsigned w) { return __uint_as_float(w); }
__device__ __forceinline__ unsigned asu(float w) { return __float_as_uint(w); }
__device__ __forceinline__ unsigned pklo(unsigned a, unsigned b) { return __builtin_amdgcn_perm(b, a, 0x05040100u); }
__device__ __forceinline__ unsigned pkhi(unsigned a, unsigned b) { return __builtin_amdgcn_perm(b, a, 0x07060302u); }
__device__ __forceinline__ float dot2(unsigned k, unsigned q, float acc) {
  return __builtin_amdgcn_fdot2_f32_bf16(__builtin_bit_cast(bf16x2, k), __builtin_bit_cast(bf16x2, q), acc, false);
}
template <int CTRL> __device__ __forceinline__ float dpp(float x) {
  return __builtin_bit_cast(float, __builtin_amdgcn_mov_dpp(__builtin_bit_cast(int, x), CTRL, 0xf, 0xf, true));
}
template <int CTRL> __device__ __forceinline__ unsigned dppu(unsigned x) { return (unsigned)__builtin_amdgcn_mov_dpp((int)x, CTRL, 0xf, 0xf, true); }
constexpr int XOR1 = 0xB1, XOR2 = 0x4E, XOR3 = 0x1B, XOR7 = 0x141, XOR8 = 0x128, ROR4 = 0x124;
__device__ __forceinline__ float tr16(float a, float b) {
  auto s = __builtin_amdgcn_permlane16_swap(asu(a), asu(b), false, false);
  return asf(s[0]) + asf(s[1]);
}
__device__ __forceinline__ float tr32(float a, float b) {
  auto s = __builtin_amdgcn_permlane32_swap(asu(a), asu(b), false, false);
  return asf(s[0]) + asf(s[1]);
}
__device__ __forceinline__ float max16(float a) {
  auto s = __builtin_amdgcn_permlane16_swap(asu(a), asu(a), false, false);
  return fmaxf(asf(s[0]), asf(s[1]));
}
__device__ __forceinline__ float max32(float a) {
  auto s = __builtin_amdgcn_permlane32_swap(asu(a), asu(a), false, false);
  return fmaxf(asf(s[0]), asf(s[1]));
}
__device__ __forceinline__ float wave_sum(float t) {
  t += dpp<XOR1>(t); t += dpp<XOR2>(t); t += dpp<ROR4>(t); t += dpp<XOR8>(t);
  t = tr16(t, t); t = tr32(t, t);
  return t;
}
__device__ __forceinline__ float red8(float (&a)[8], int lane) {
#pragma unroll
  for (int h = 0; h < 4; ++h) { const float t1 = a[h] + dpp<XOR8>(a[h]), t2 = a[h + 4] + dpp<XOR8>(a[h + 4]); a[h] = (lane & 8) ? t2 : t1; }
  a[0] = tr16(a[0], a[2]); a[1] = tr16(a[1], a[3]);
  return tr32(a[0], a[1]);
}
#define VMWAIT(n) asm volatile("s_waitcnt vmcnt(%0)" :: "n"(n) : "memory")
#define LGKM0() asm volatile("s_waitcnt lgkmcnt(0)" ::: "memory")
#define LDNT(r, off) __builtin_amdgcn_raw_buffer_load_b128((r), (off), 0, 2)
#define LDNT16(r, off) __builtin_amdgcn_raw_buffer_load_b16((r), (off), 0, 2)
#define LDSC(r, off, so) __builtin_amdgcn_raw_buffer_load_b128((r), (off), (so), 16)
#define LDSC8(r, off, so) __builtin_amdgcn_raw_buffer_load_b64((r), (off), (so), 16)
#define STSC(v, r, off) __builtin_amdgcn_raw_buffer_store_b128((v), (r), (off), 0, 16)
#define STSC8(v, r, off) __builtin_amdgcn_raw_buffer_store_b64((v), (r), (off), 0, 16)
#define SB() __builtin_amdgcn_sched_barrier(0)
#define PIN8(a) asm volatile("" : "+v"(a[0]), "+v"(a[1]), "+v"(a[2]), "+v"(a[3]), "+v"(a[4]), "+v"(a[5]), "+v"(a[6]), "+v"(a[7]))

template <bool SLEEP> __device__ __forceinline__ void lds_wait_ge(volatile LAS unsigned* p, unsigned v) {
#pragma unroll 1
  for (unsigned n = 0; n < SPIN_CAP; ++n) { if (*p >= v) break; if (SLEEP) __builtin_amdgcn_s_sleep(1); }
  asm volatile("" ::: "memory");
}
__device__ __forceinline__ void lds_arrive(LAS unsigned* p, int lane) {
  asm volatile("" ::: "memory");
  if (lane == 0) (void)__hip_atomic_fetch_add(p, 1u, __ATOMIC_RELAXED, __HIP_MEMORY_SCOPE_WORKGROUP);
  asm volatile("" ::: "memory");
}

__device__ __forceinline__ void layernorm9(float (&v)[9]) {
  float s = ((v[0] + v[1]) + (v[2] + v[3])) + ((v[4] + v[5]) + (v[6] + v[7])) + v[8];
  const float mean = wave_sum(s) * (1.f / D);
  float s2 = 0.f, s3 = 0.f;
#pragma unroll
  for (int k = 0; k < 8; k += 2) { const float d = v[k] - mean, e = v[k + 1] - mean; s2 = fmaf(d, d, s2); s3 = fmaf(e, e, s3); }
  { const float d = v[8] - mean; s2 = fmaf(d, d, s2); }
  const float var = rbf(wave_sum(s2 + s3) * (1.f / D));
  const float mb = rbf(mean);
  const float den = rbf(__builtin_amdgcn_sqrtf(rbf(var + 1e-5f)));
  const float inv = __builtin_amdgcn_rcpf(den);
#pragma unroll
  for (int k = 0; k < 9; ++k) {
    const float n = rbf(v[k] - mb);
    float qv = n * inv;
    qv = fmaf(fmaf(-qv, den, n), inv, qv);
    v[k] = rbf(qv);
  }
}
__device__ __forceinline__ void store9(LAS unsigned* dst, const float (&v)[9], int lane) {
  *(LAS v4u*)(dst + 4 * lane) = v4u{pkhi(asu(v[0]), asu(v[1])), pkhi(asu(v[2]), asu(v[3])), pkhi(asu(v[4]), asu(v[5])), pkhi(asu(v[6]), asu(v[7]))};
  ((LAS u16*)dst)[512 + lane] = (u16)(asu(v[8]) >> 16);
}

#define WSTAMP(k) do {} while (0)
#define WDUMP() do {} while (0)

__global__ void __launch_bounds__(NTHR) fused_decode(const u16* __restrict__ x, const u16* __restrict__ kc, const u16* __restrict__ vc,
                                                     const u16* __restrict__ wq, const u16* __restrict__ wo, const u16* __restrict__ w1,
                                                     const u16* __restrict__ b1, const u16* __restrict__ w2, const u16* __restrict__ b2,
                                                     u16* __restrict__ out, unsigned char* ws) {
  unsigned long long pc0;
  asm volatile("s_getpc_b64 %0" : "=s"(pc0));
  extern __shared__ __attribute__((aligned(16))) float smem[];
  LAS float* L = (LAS float*)smem;
  LAS unsigned* LU = (LAS unsigned*)smem;
  volatile LAS unsigned* FL = (volatile LAS unsigned*)(LU + L_FLAG);
  const int t = threadIdx.x, lane = t & 63;
  const int wave = __builtin_amdgcn_readfirstlane(t >> 6);
  const int u = blockIdx.x, g = u >> 4, gi = u & 15;
  auto mk = [](const void* p, unsigned bytes) { return __builtin_amdgcn_make_buffer_rsrc((void*)p, 0, bytes, 0x00020000); };
  const auto rws = mk(ws, WS_END);
  const auto rx = mk(x, D * 2);

  if (wave == 0) {
    const int xcd = u & 7, jj = u >> 3;
    const int lg = (jj >> 2) * 8 + xcd;
    const bool is_red = lg < 36;
    const int rcol = 16 * lg + 4 * (jj & 3);
    const auto rc = mk((const void*)(pc0 & ~255ull), CODE_KB * 1024);
    const v4u cpf = __builtin_amdgcn_raw_buffer_load_b128(rc, (jj % CODE_KB) * 1024 + lane * 16, 0, 0);
    const auto rb2 = mk(b2, D * 2);
    const int oc = 2 * (lane & 1) + (lane >> 5);
    const unsigned xrr = __builtin_amdgcn_raw_buffer_load_b16(rx, is_red ? (rcol + oc) * 2 : OOB, 0, 0);
    const unsigned b2r = __builtin_amdgcn_raw_buffer_load_b16(rb2, is_red ? (rcol + oc) * 2 : OOB, 0, 0);
    if (lane < 32) LU[L_FLAG + lane] = 0u;
    LGKM0();
    __builtin_amdgcn_s_barrier();
    SB();
    asm volatile("" :: "v"(cpf));
    lds_wait_ge<true>(FL + F_QPUB, 1);
    const unsigned tag = __builtin_amdgcn_readfirstlane(FL[F_TAG]);
    __builtin_amdgcn_s_sleep(DLY_Q);
    WSTAMP(1);
    {
      v4u qv[8];
      const unsigned qoff = WS_Q + g * 8192 + lane * 16;
#pragma unroll 1
      for (unsigned n = 0; n < SPIN_CAP; ++n) {
        asm volatile("" ::: "memory");
        unsigned bad = 0;
#pragma unroll
        for (int k = 0; k < 8; ++k) qv[k] = LDSC(rws, (NOPOLL & 1) ? OOB : qoff, k * 1024);
#pragma unroll
        for (int k = 0; k < 8; ++k) bad |= (qv[k].y ^ tag) | (qv[k].w ^ tag);
        if (__all(bad == 0) || (NOPOLL & 1)) break;
      }
      float s[8];
#pragma unroll
      for (int a = 0; a < 4; ++a) { s[2 * a] = asf(qv[2 * a].x) + asf(qv[2 * a + 1].x); s[2 * a + 1] = asf(qv[2 * a].z) + asf(qv[2 * a + 1].z); }
      const float r0 = tr32(s[0], s[4]), r1_ = tr32(s[1], s[5]), r2_ = tr32(s[2], s[6]), r3 = tr32(s[3], s[7]);
      const int a0 = (lane >> 5) * 2;
      LU[L_QB + a0 * 32 + (lane & 31)] = pkhi(asu(rbf(r0)), asu(rbf(r1_)));
      LU[L_QB + (a0 + 1) * 32 + (lane & 31)] = pkhi(asu(rbf(r2_)), asu(rbf(r3)));
      asm volatile("" ::: "memory");
      if (lane == 0) FL[F_Q] = 1u;
      WSTAMP(2);
    }
    lds_wait_ge<false>(FL + F_OCNT, 8);
    WSTAMP(3);
    {
      v2f oa = *(const LAS v2f*)(L + L_OP + 2 * lane), ob = *(const LAS v2f*)(L + L_OP + 128 + 2 * lane);
#pragma unroll
      for (int w = 1; w < 8; ++w) { oa += *(const LAS v2f*)(L + L_OP + w * 256 + 2 * lane); ob += *(const LAS v2f*)(L + L_OP + w * 256 + 128 + 2 * lane); }
      const int hd = lane & 1;
      const v4f la = *(const LAS v4f*)(L + L_LW + hd * 8), lb = *(const LAS v4f*)(L + L_LW + hd * 8 + 4);
      const v4f ma = *(const LAS v4f*)(L + L_MW + hd * 8), mb = *(const LAS v4f*)(L + L_MW + hd * 8 + 4);
      const float Ls = ((la.x + la.y) + (la.z + la.w)) + ((lb.x + lb.y) + (lb.z + lb.w));
      const float M = fmaxf(fmaxf(fmaxf(ma.x, ma.y), fmaxf(ma.z, ma.w)), fmaxf(fmaxf(mb.x, mb.y), fmaxf(mb.z, mb.w)));
      STSC((v4u{asu(oa.x), tag, asu(oa.y), tag}), rws, WS_O + u * 2048 + lane * 16);
      STSC((v4u{asu(ob.x), tag, asu(ob.y), tag}), rws, WS_O + u * 2048 + 1024 + lane * 16);
      STSC((v4u{asu(M), tag, asu(Ls), tag}), rws, lane < 8 ? WS_M + u * 128 + lane * 16 : OOB);
    }
    WSTAMP(4);
    __builtin_amdgcn_s_sleep(DLY_O);
    {
      const unsigned ooff = WS_O + (16 * g + (lane >> 4)) * 2048 + (16 * gi + (lane & 15)) * 8;
      const unsigned moff = WS_M + (16 * g + (lane >> 4)) * 128 + (gi >> 3) * 16;
      v2u ov[4]; v4u ml[4];
#pragma unroll 1
      for (unsigned n = 0; n < SPIN_CAP; ++n) {
        asm volatile("" ::: "memory");
        unsigned bad = 0;
#pragma unroll
        for (int k = 0; k < 4; ++k) { ov[k] = LDSC8(rws, (NOPOLL & 2) ? OOB : ooff, k * 8192); ml[k] = LDSC(rws, (NOPOLL & 2) ? OOB : moff, k * 512); }
#pragma unroll
        for (int k = 0; k < 4; ++k) bad |= (ov[k].y ^ tag) | (ml[k].y ^ tag) | (ml[k].w ^ tag);
        if (__all(bad == 0) || (NOPOLL & 2)) break;
      }
      WSTAMP(5);
      float M = fmaxf(fmaxf(asf(ml[0].x), asf(ml[1].x)), fmaxf(asf(ml[2].x), asf(ml[3].x)));
      M = max16(M); M = max32(M);
      float num = 0.f, den = 0.f;
#pragma unroll
      for (int k = 0; k < 4; ++k) { const float sc = __builtin_amdgcn_exp2f((asf(ml[k].x) - M) * LOG2E); num = fmaf(sc, asf(ov[k].x), num); den = fmaf(sc, asf(ml[k].z), den); }
      num = tr16(num, num); den = tr16(den, den); num = tr32(num, num); den = tr32(den, den);
      const float o = rbf(num * __builtin_amdgcn_rcpf(den));
      {
        const int d = lane & 15, m0 = 2 * (lane >> 4);
        LAS u16* fp = (LAS u16*)(LU + L_FR) + ((d >> 2) * 64 + (d & 3) * 16 + m0) * 8 + m0;
        fp[0] = (u16)(asu(o) >> 16); fp[9] = (u16)(asu(o) >> 16);
      }
      asm volatile("" ::: "memory");
      if (lane == 0) FL[F_CO] = 1u;
      WSTAMP(6);
    }
    if (is_red) {
      const unsigned off = WS_P + ((lane >> 1) * 576 + rcol + 2 * (lane & 1)) * 8;
      const float xb2 = lo16(xrr) + lo16(b2r);
      lds_wait_ge<false>(FL + F_PPUB, 1);
      __builtin_amdgcn_s_sleep(DLY_P);
      v4u pv[8];
#pragma unroll 1
      for (unsigned n = 0; n < SPIN_CAP; ++n) {
        asm volatile("" ::: "memory");
        unsigned bad = 0;
#pragma unroll
        for (int k = 0; k < 8; ++k) pv[k] = LDSC(rws, (NOPOLL & 4) ? OOB : off, k * (32 * 576 * 8));
#pragma unroll
        for (int k = 0; k < 8; ++k) bad |= (pv[k].y ^ tag) | (pv[k].w ^ tag);
        if (__all(bad == 0) || (NOPOLL & 4)) break;
      }
      float s0 = ((asf(pv[0].x) + asf(pv[1].x)) + (asf(pv[2].x) + asf(pv[3].x))) + ((asf(pv[4].x) + asf(pv[5].x)) + (asf(pv[6].x) + asf(pv[7].x)));
      float s1 = ((asf(pv[0].z) + asf(pv[1].z)) + (asf(pv[2].z) + asf(pv[3].z))) + ((asf(pv[4].z) + asf(pv[5].z)) + (asf(pv[6].z) + asf(pv[7].z)));
      s0 = tr32(s0, s1);
      s0 += dpp<XOR2>(s0); s0 += dpp<ROR4>(s0); s0 += dpp<XOR8>(s0); s0 = tr16(s0, s0);
      if ((lane & 30) == 0) out[rcol + oc] = (u16)(asu(rbf(s0 + xb2)) >> 16);
      if (u == 0 && lane == 0) __builtin_amdgcn_raw_buffer_store_b32(tag, rws, WS_E, 0, 16);
      WSTAMP(7);
    }
    WDUMP();
    return;
  }
  if (wave == 10) {
    if (u < 8) asm volatile("buffer_wbl2 sc1" ::: "memory");
    const auto rb1 = mk(b1, F * 2);
    const unsigned b1r = LDNT16(rb1, (lane & 31) < 18 ? (18 * u + (lane & 31)) * 2 : OOB);
    __builtin_amdgcn_s_barrier();
    lds_wait_ge<true>(FL + F_QPUB, 1);
    const unsigned tag = __builtin_amdgcn_readfirstlane(FL[F_TAG]);
    {
      const v4u xa = *(const LAS v4u*)(LU + L_XN + 4 * lane);
      const unsigned xb = ((const LAS u16*)(LU + L_XN))[512 + lane];
      float v[9];
#pragma unroll
      for (int k = 0; k < 4; ++k) { v[2 * k] = lo16(xa[k]); v[2 * k + 1] = hi16(xa[k]); }
      v[8] = lo16(xb);
      layernorm9(v);
      store9(LU + L_Y, v, lane);
#pragma unroll
      for (int it = 0; it < 5; ++it) {
        const int idx = it * 64 + lane, n = idx < 288 ? idx >> 3 : 35, m = idx & 7;
        ((LAS u16*)(LU + L_FY))[((n >> 2) * 64 + (n & 3) * 16 + m) * 8 + m] = ((const LAS u16*)(LU + L_Y))[36 * gi + n];
      }
    }
    asm volatile("" ::: "memory");
    if (lane == 0) FL[F_Y] = 1u;
    WSTAMP(1);
    const unsigned hoff = (lane & 31) < 18 ? WS_H + ((16 * g + (lane >> 5)) * 288 + 18 * gi + (lane & 31)) * 8 : OOB;
    const unsigned tagx = (lane & 31) < 18 ? tag : 0u;
    lds_wait_ge<true>(FL + F_HPUB, 3);
    __builtin_amdgcn_s_sleep(DLY_H);
    WSTAMP(2);
    v2u hv[8];
#pragma unroll 1
    for (unsigned n = 0; n < SPIN_CAP; ++n) {
      asm volatile("" ::: "memory");
      unsigned bad = 0;
#pragma unroll
      for (int k = 0; k < 8; ++k) hv[k] = LDSC8(rws, (NOPOLL & 2) ? OOB : hoff, k * (2 * 288 * 8));
#pragma unroll
      for (int k = 0; k < 8; ++k) bad |= hv[k].y ^ tagx;
      if (__all(bad == 0) || (NOPOLL & 2)) break;
    }
    WSTAMP(3);
    float pre = ((asf(hv[0].x) + asf(hv[1].x)) + (asf(hv[2].x) + asf(hv[3].x))) + ((asf(hv[4].x) + asf(hv[5].x)) + (asf(hv[6].x) + asf(hv[7].x)));
    pre = tr32(pre, pre);
    const float tt = rbf(rbf(pre) + lo16(b1r));
    const float inner = 0.7978845608028654f * (tt + 0.044715f * tt * tt * tt);
    const float th = 1.f - 2.f * __builtin_amdgcn_rcpf(__builtin_amdgcn_exp2f(inner * (2.f * LOG2E)) + 1.f);
    const float h = rbf(0.5f * tt * (1.f + th));
    if ((lane & 31) < 18) {
      const int n = 16 + (lane & 31), m0 = 4 * (lane >> 5);
      LAS u16* fp = (LAS u16*)(LU + L_FR) + ((n >> 2) * 64 + (n & 3) * 16 + m0) * 8 + m0;
      fp[0] = (u16)(asu(h) >> 16); fp[9] = (u16)(asu(h) >> 16); fp[18] = (u16)(asu(h) >> 16); fp[27] = (u16)(asu(h) >> 16);
    }
    asm volatile("" ::: "memory");
    if (lane == 0) FL[F_CH] = 1u;
    WSTAMP(4);
    WDUMP();
    return;
  }

  if (wave >= 2 && wave < 10) {
    const int ww = wave - 2;
    const size_t kvb = ((size_t)g * SK + (SK - W) + gi * 256) * 128;
    const auto rk = mk(kc + kvb, 65536), rv = mk(vc + kvb, 65536);
    v4u K[8], V[8];
    const unsigned kvoff = ILV ? ww * 1024 + lane * 16 : (ww * 32 * 128 + lane * 8) * 2;
    constexpr int JS = ILV ? 8192 : 1024;
    __builtin_amdgcn_s_sleep(KSLEEP);
#pragma unroll
    for (int j = 0; j < 8; ++j) { if (j == WIN) __builtin_amdgcn_s_barrier(); if (j >= WIN) VMWAIT(WIN - 1); K[j] = __builtin_amdgcn_raw_buffer_load_b128(rk, kvoff, j * JS, 2); }
#pragma unroll
    for (int j = 0; j < 8; ++j) { VMWAIT(WIN - 1); V[j] = __builtin_amdgcn_raw_buffer_load_b128(rv, kvoff, j * JS, 2); if (j == KVI_AT) lds_arrive(LU + L_FLAG + F_KVI, lane); }
    SB();
    WSTAMP(1);
    lds_wait_ge<true>(FL + F_Q, 1);
    WSTAMP(2);
    const int c = lane & 15;
    unsigned pp;
    {
      const v4u q0 = *(const LAS v4u*)(LU + L_QB + 4 * c), q1 = *(const LAS v4u*)(LU + L_QB + 64 + 4 * c);
      float xs[16];
#pragma unroll
      for (int j = 0; j < 8; ++j) {
        float d0 = 0.f, d1 = 0.f;
#pragma unroll
        for (int k = 0; k < 4; ++k) { d0 = dot2(K[j][k], q0[k], d0); d1 = dot2(K[j][k], q1[k], d1); }
        xs[2 * j] = d0; xs[2 * j + 1] = d1;
      }
#define TR_STEP(HALF, CTRL, BIT) _Pragma("unroll") for (int h = 0; h < (HALF); ++h) { \
      const float lo_ = xs[h], hi_ = xs[h + (HALF)]; const float t1_ = lo_ + dpp<CTRL>(lo_), t2_ = hi_ + dpp<CTRL>(hi_); xs[h] = (c & (BIT)) ? t2_ : t1_; }
      TR_STEP(8, XOR8, 8) TR_STEP(4, XOR7, 4) TR_STEP(2, XOR2, 2) TR_STEP(1, XOR1, 1)
#undef TR_STEP
      const float s = rbf(rbf(xs[0]) * 0.08838834764831845f);
      float m = s;
      m = fmaxf(m, dpp<XOR2>(m)); m = fmaxf(m, dpp<ROR4>(m)); m = fmaxf(m, dpp<XOR8>(m)); m = max16(m); m = max32(m);
      if (lane < 2) L[L_MW + lane * 8 + ww] = m;
      lds_arrive(LU + L_FLAG + F_MCNT, lane);
      lds_wait_ge<false>(FL + F_MCNT, 8);
      WSTAMP(3);
      const v4f ma = *(const LAS v4f*)(L + L_MW + (lane & 1) * 8), mb = *(const LAS v4f*)(L + L_MW + (lane & 1) * 8 + 4);
      m = fmaxf(fmaxf(fmaxf(ma.x, ma.y), fmaxf(ma.z, ma.w)), fmaxf(fmaxf(mb.x, mb.y), fmaxf(mb.z, mb.w)));
      const float p = __builtin_amdgcn_exp2f((s - m) * LOG2E);
      float l = p;
      l += dpp<XOR2>(l); l += dpp<ROR4>(l); l += dpp<XOR8>(l); l = tr16(l, l); l = tr32(l, l);
      if (lane < 2) L[L_LW + lane * 8 + ww] = l;
      const unsigned pb = asu(rbf(p));
      pp = pkhi(pb, pb);
    }
    v4u msk;
#pragma unroll
    for (int k = 0; k < 4; ++k) msk[k] = ((c & 7) >> 1) == k ? ((c & 1) ? 0xffff0000u : 0x0000ffffu) : 0u;
    v4f acc = {0.f, 0.f, 0.f, 0.f};
#define PV_STEP(j) { \
      int pj = __builtin_amdgcn_update_dpp(0, (int)pp, 0x150 + 2 * j, 0xf, 0x3, false); \
      pj = __builtin_amdgcn_update_dpp(pj, (int)pp, 0x150 + 2 * j + 1, 0xf, 0xc, false); \
      const v4u af = {msk[0] & pj, msk[1] & pj, msk[2] & pj, msk[3] & pj}; \
      acc = __builtin_amdgcn_mfma_f32_16x16x32_bf16(__builtin_bit_cast(bf16x8, af), __builtin_bit_cast(bf16x8, V[j]), acc, 0, 0, 0); }
    PV_STEP(0) PV_STEP(1) PV_STEP(2) PV_STEP(3) PV_STEP(4) PV_STEP(5) PV_STEP(6) PV_STEP(7)
#undef PV_STEP
    *(LAS v4f*)(L + L_OP + ww * 256 + (lane >> 5) * 128 + 8 * c + 4 * ((lane >> 4) & 1)) = acc;
    lds_arrive(LU + L_FLAG + F_OCNT, lane);
    WSTAMP(4);
    WDUMP();
    return;
  }

  if (wave == 1) {
    const auto rq = mk(wq, 576 * 4096 * 2);
    const int qa = gi >> 2, qr = gi & 3;
    const v4u xa = __builtin_amdgcn_raw_buffer_load_b128(rx, lane * 16, 0, 0);
    const unsigned xb = __builtin_amdgcn_raw_buffer_load_b16(rx, 1024 + lane * 2, 0, 0);
    const unsigned e = __builtin_amdgcn_raw_buffer_load_b32(rws, WS_E, 0, 16);
    v4u WQ[18];
    const unsigned offq = ((144 * qr + 2 * (lane >> 3)) * 4096 + 256 * g + 64 * qa + 8 * (lane & 7)) * 2;
#pragma unroll
    for (int i = 0; i < 18; ++i) WQ[i] = __builtin_amdgcn_raw_buffer_load_b128(rq, offq, ((i >> 1) * 16 + (i & 1)) * 8192, 2);
    __builtin_amdgcn_s_barrier();
    SB();
    float v[9];
#pragma unroll
    for (int k = 0; k < 4; ++k) { v[2 * k] = lo16(xa[k]); v[2 * k + 1] = hi16(xa[k]); }
    v[8] = lo16(xb);
    layernorm9(v);
    store9(LU + L_XN, v, lane);
    WSTAMP(1);
    float a[8];
#pragma unroll
    for (int k = 0; k < 8; ++k) a[k] = 0.f;
#pragma unroll
    for (int i = 0; i < 9; ++i) {
      const unsigned xp = LU[L_XN + 72 * qr + 8 * i + (lane >> 3)];
#pragma unroll
      for (int c = 0; c < 4; ++c) {
        a[2 * c] = dot2(pklo(WQ[2 * i][c], WQ[2 * i + 1][c]), xp, a[2 * c]);
        a[2 * c + 1] = dot2(pkhi(WQ[2 * i][c], WQ[2 * i + 1][c]), xp, a[2 * c + 1]);
      }
    }
    const float r = red8(a, lane);
    const unsigned tg = __builtin_amdgcn_readfirstlane(e) + 1u;
    const int col = 8 * (lane & 7) + ((lane >> 3) & 1) * 4 + ((lane >> 4) & 1) * 2 + (lane >> 5);
    STSC8((v2u{asu(r), tg}), rws, WS_Q + (u * 64 + col) * 8);
    asm volatile("" ::: "memory");
    if (lane == 0) { FL[F_TAG] = tg; FL[F_QPUB] = 1u; }
    WSTAMP(2);
  } else {
#pragma unroll
    for (int k = 0; k < 5; ++k) { const int i = (k * 4 + (wave - 11)) * 64 + lane; if (i < 18 * 64) *(LAS v4u*)(LU + L_FR + 4 * i) = v4u{0u, 0u, 0u, 0u}; }
    LGKM0();
    __builtin_amdgcn_s_barrier();
  }
  {
    const int r = lane >> 4, c = lane & 15;
    const auto ro = mk(wo + (size_t)u * 16 * D, 16 * D * 2), r2 = mk(w2 + (size_t)u * 18 * D, 18 * D * 2);
    v4u W1[9], WF[9];
    const bool has1 = wave >= 11 && wave < 14;
    if (has1) {
      const auto r1 = mk(w1, 576 * F * 2);
      const int hw = wave - 11;
      const unsigned off1 = c < 12 ? ((36 * gi + r) * F + 288 * g + 96 * hw + 8 * c) * 2 : OOB;
      lds_wait_ge<true>(FL + F_KVI, W1_AT + hw);
      WSTAMP(3);
#pragma unroll
      for (int k = 0; k < 9; ++k) W1[k] = __builtin_amdgcn_raw_buffer_load_b128(r1, off1, k * (4 * F * 2), 2);
      lds_arrive(LU + L_FLAG + F_W1I, lane);
    }
    const int cb = wave == 1 ? 512 : 128 * (wave - 11);
    const unsigned off2 = (wave != 1 || c < 8) ? (r * D + cb + 8 * c) * 2 : OOB;
    lds_wait_ge<true>(FL + F_KVI, WF_AT);
    lds_wait_ge<true>(FL + F_W1I, 3);
    WSTAMP(4);
#pragma unroll
    for (int k = 0; k < 4; ++k) WF[k] = __builtin_amdgcn_raw_buffer_load_b128(ro, off2, k * (4 * D * 2), 2);
#pragma unroll
    for (int k = 0; k < 5; ++k) WF[4 + k] = LDNT(r2, off2 + k * (4 * D * 2));
    SB();
    lds_wait_ge<true>(FL + F_Y, 1);
    const unsigned tag = FL[F_TAG];
    const int cl = 8 * c + 4 * (r & 1) + 2 * (r >> 1);
    if (has1) {
      v4f acc = {0.f, 0.f, 0.f, 0.f};
#pragma unroll
      for (int k = 0; k < 9; ++k)
        acc = __builtin_amdgcn_mfma_f32_16x16x32_bf16(__builtin_bit_cast(bf16x8, *(const LAS v4u*)(LU + L_FY + k * 256 + 4 * lane)), __builtin_bit_cast(bf16x8, W1[k]), acc, 0, 0, 0);
      const auto s0 = __builtin_amdgcn_permlane32_swap(asu(acc[0]), asu(acc[2]), false, false);
      const auto s1 = __builtin_amdgcn_permlane32_swap(asu(acc[1]), asu(acc[3]), false, false);
      STSC((v4u{s0[0], tag, s1[0], tag}), rws, c < 12 ? WS_H + (u * 288 + 96 * (wave - 11) + cl) * 8 : OOB);
      lds_arrive(LU + L_FLAG + F_HPUB, lane);
      WSTAMP(5);
    }
    const unsigned poff = (wave != 1 || c < 8) ? WS_P + (u * 576 + cb + cl) * 8 : OOB;
    v4f acc = {0.f, 0.f, 0.f, 0.f}, acc2 = {0.f, 0.f, 0.f, 0.f};
    lds_wait_ge<false>(FL + F_CO, 1);
#pragma unroll
    for (int k = 0; k < 4; ++k)
      acc = __builtin_amdgcn_mfma_f32_16x16x32_bf16(__builtin_bit_cast(bf16x8, *(const LAS v4u*)(LU + L_FR + k * 256 + 4 * lane)), __builtin_bit_cast(bf16x8, WF[k]), acc, 0, 0, 0);
    asm volatile("" : "+v"(acc));
    lds_wait_ge<false>(FL + F_CH, 1);
#pragma unroll
    for (int k = 4; k < 9; ++k) {
      v4f& a = (k & 1) ? acc2 : acc;
      a = __builtin_amdgcn_mfma_f32_16x16x32_bf16(__builtin_bit_cast(bf16x8, *(const LAS v4u*)(LU + L_FR + k * 256 + 4 * lane)), __builtin_bit_cast(bf16x8, WF[k]), a, 0, 0, 0);
    }
    acc += acc2;
    const auto s0 = __builtin_amdgcn_permlane32_swap(asu(acc[0]), asu(acc[2]), false, false);
    const auto s1 = __builtin_amdgcn_permlane32_swap(asu(acc[1]), asu(acc[3]), false, false);
    STSC((v4u{s0[0], tag, s1[0], tag}), rws, poff);
    if (lane == 0) FL[F_PPUB] = 1u;
    WSTAMP(7);
  }
  WDUMP();
}

extern "C" void kernel_launch(void* const* d_in, const int* in_sizes, int n_in,
                              void* d_out, int out_size, void* d_ws, size_t ws_size,
                              hipStream_t stream) {
  (void)hipFuncSetAttribute((const void*)fused_decode, hipFuncAttributeMaxDynamicSharedMemorySize, LDS_BYTES);
  fused_decode<<<256, NTHR, LDS_BYTES, stream>>>((const u16*)d_in[0], (const u16*)d_in[1], (const u16*)d_in[2], (const u16*)d_in[3],
                                                 (const u16*)d_in[4], (const u16*)d_in[6], (const u16*)d_in[7], (const u16*)d_in[8],
                                                 (const u16*)d_in[9], (u16*)d_out, (unsigned char*)d_ws);
}
```

```cpp
#define MODE 0
#include <hip/hip_runtime.h>
#include <hip/hip_bf16.h>
#include <cstdint>

typedef unsigned short u16;
typedef unsigned v4u __attribute__((ext_vector_type(4)));
typedef unsigned v2u __attribute__((ext_vector_type(2)));
typedef float v4f __attribute__((ext_vector_type(4)));
typedef float v2f __attribute__((ext_vector_type(2)));
typedef __bf16 bf16x2 __attribute__((ext_vector_type(2)));
typedef short bf16x8 __attribute__((ext_vector_type(8)));
#define LAS __attribute__((address_space(3)))

#define WIN 4
#define WWIN 7
#define NOPOLL 0
#define ILV 1
#define KSLEEP 4
#define W1_AT 2
#define WF_AT 8
#define STAG 1
#define KVI_AT 7
#define CODE_KB 13
#define DLY_Q 30
#define DLY_O 16
#define DLY_H 16
#define DLY_P 16

constexpr int D = 576, SK = 65536, W = 4096, F = 4608;
constexpr int NTHR = 960;
constexpr unsigned OOB = 0x80000000u;
constexpr unsigned WS_E = 0;
constexpr unsigned WS_Q = 4096;
constexpr unsigned WS_H = WS_Q + 256 * 64 * 8;
constexpr unsigned WS_O = WS_H + 256 * 288 * 8;
constexpr unsigned WS_M = WS_O + 256 * 256 * 8;
constexpr unsigned WS_P = WS_M + 256 * 128;
constexpr unsigned WS_END = WS_P + 256 * 576 * 8;
constexpr unsigned WS_DBG = 8u << 20;
constexpr int L_XN = 0, L_Y = L_XN + 288, L_QB = L_Y + 288, L_MW = L_QB + 128, L_LW = L_MW + 16, L_CO = L_LW + 16,
              L_OP = L_CO + 32, L_FLAG = L_OP + 8 * 256, L_FR = L_FLAG + 32, L_FY = L_FR + 9 * 256, L_END = L_FY + 9 * 256;
constexpr int LDS_BYTES = 96 * 1024;
static_assert(L_END * 4 <= LDS_BYTES, "lds");
enum { F_CH = 11, F_W1I = 10, F_KVI = 9, F_Y = 0, F_QPUB, F_Q, F_MCNT, F_OCNT, F_HPUB, F_CO, F_PPUB, F_TAG };
constexpr unsigned SPIN_CAP = 1u << 22;
constexpr float LOG2E = 1.4426950408889634f;

__device__ __forceinline__ float rbf(float f) {
  unsigned u = __float_as_uint(f);
  u = (u + 0x7fffu + ((u >> 16) & 1u)) & 0xffff0000u;
  return __uint_as_float(u);
}
__device__ __forceinline__ float lo16(unsigned w) { return __uint_as_float(w << 16); }
__device__ __forceinline__ float hi16(unsigned w) { return __uint_as_float(w & 0xffff0000u); }
__device__ __forceinline__ float asf(unsigned w) { return __uint_as_float(w); }
__device__ __forceinline__ unsigned asu(float w) { return __float_as_uint(w); }
__device__ __forceinline__ unsigned pklo(unsigned a, unsigned b) { return __builtin_amdgcn_perm(b, a, 0x05040100u); }
__device__ __forceinline__ unsigned pkhi(unsigned a, unsigned b) { return __builtin_amdgcn_perm(b, a, 0x07060302u); }
__device__ __forceinline__ float dot2(unsigned k, unsigned q, float acc) {
  return __builtin_amdgcn_fdot2_f32_bf16(__builtin_bit_cast(bf16x2, k), __builtin_bit_cast(bf16x2, q), acc, false);
}
template <int CTRL> __device__ __forceinline__ float dpp(float x) {
  return __builtin_bit_cast(float, __builtin_amdgcn_mov_dpp(__builtin_bit_cast(int, x), CTRL, 0xf, 0xf, true));
}
template <int CTRL> __device__ __forceinline__ unsigned dppu(unsigned x) { return (unsigned)__builtin_amdgcn_mov_dpp((int)x, CTRL, 0xf, 0xf, true); }
constexpr int XOR1 = 0xB1, XOR2 = 0x4E, XOR3 = 0x1B, XOR7 = 0x141, XOR8 = 0x128, ROR4 = 0x124;
__device__ __forceinline__ float tr16(float a, float b) {
  auto s = __builtin_amdgcn_permlane16_swap(asu(a), asu(b), false, false);
  return asf(s[0]) + asf(s[1]);
}
__device__ __forceinline__ float tr32(float a, float b) {
  auto s = __builtin_amdgcn_permlane32_swap(asu(a), asu(b), false, false);
  return asf(s[0]) + asf(s[1]);
}
__device__ __forceinline__ float max16(float a) {
  auto s = __builtin_amdgcn_permlane16_swap(asu(a), asu(a), false, false);
  return fmaxf(asf(s[0]), asf(s[1]));
}
__device__ __forceinline__ float max32(float a) {
  auto s = __builtin_amdgcn_permlane32_swap(asu(a), asu(a), false, false);
  return fmaxf(asf(s[0]), asf(s[1]));
}
__device__ __forceinline__ float wave_sum(float t) {
  t += dpp<XOR1>(t); t += dpp<XOR2>(t); t += dpp<ROR4>(t); t += dpp<XOR8>(t);
  t = tr16(t, t); t = tr32(t, t);
  return t;
}
__device__ __forceinline__ float red8(float (&a)[8], int lane) {
#pragma unroll
  for (int h = 0; h < 4; ++h) { const float t1 = a[h] + dpp<XOR8>(a[h]), t2 = a[h + 4] + dpp<XOR8>(a[h + 4]); a[h] = (lane & 8) ? t2 : t1; }
  a[0] = tr16(a[0], a[2]); a[1] = tr16(a[1], a[3]);
  return tr32(a[0], a[1]);
}
#define VMWAIT(n) asm volatile("s_waitcnt vmcnt(%0)" :: "n"(n) : "memory")
#define LGKM0() asm volatile("s_waitcnt lgkmcnt(0)" ::: "memory")
#define LDNT(r, off) __builtin_amdgcn_raw_buffer_load_b128((r), (off), 0, 2)
#define LDNT16(r, off) __builtin_amdgcn_raw_buffer_load_b16((r), (off), 0, 2)
#define LDSC(r, off, so) __builtin_amdgcn_raw_buffer_load_b128((r), (off), (so), 16)
#define LDSC8(r, off, so) __builtin_amdgcn_raw_buffer_load_b64((r), (off), (so), 16)
#define STSC(v, r, off) __builtin_amdgcn_raw_buffer_store_b128((v), (r), (off), 0, 16)
#define STSC8(v, r, off) __builtin_amdgcn_raw_buffer_store_b64((v), (r), (off), 0, 16)
#define SB() __builtin_amdgcn_sched_barrier(0)
#define PIN8(a) asm volatile("" : "+v"(a[0]), "+v"(a[1]), "+v"(a[2]), "+v"(a[3]), "+v"(a[4]), "+v"(a[5]), "+v"(a[6]), "+v"(a[7]))

template <bool SLEEP> __device__ __forceinline__ void lds_wait_ge(volatile LAS unsigned* p, unsigned v) {
#pragma unroll 1
  for (unsigned n = 0; n < SPIN_CAP; ++n) { if (*p >= v) break; if (SLEEP) __builtin_amdgcn_s_sleep(1); }
  asm volatile("" ::: "memory");
}
__device__ __forceinline__ void lds_arrive(LAS unsigned* p, int lane) {
  asm volatile("" ::: "memory");
  if (lane == 0) (void)__hip_atomic_fetch_add(p, 1u, __ATOMIC_RELAXED, __HIP_MEMORY_SCOPE_WORKGROUP);
  asm volatile("" ::: "memory");
}

__device__ __forceinline__ void layernorm9(float (&v)[9]) {
  float s = ((v[0] + v[1]) + (v[2] + v[3])) + ((v[4] + v[5]) + (v[6] + v[7])) + v[8];
  const float mean = wave_sum(s) * (1.f / D);
  float s2 = 0.f, s3 = 0.f;
#pragma unroll
  for (int k = 0; k < 8; k += 2) { const float d = v[k] - mean, e = v[k + 1] - mean; s2 = fmaf(d, d, s2); s3 = fmaf(e, e, s3); }
  { const float d = v[8] - mean; s2 = fmaf(d, d, s2); }
  const float var = rbf(wave_sum(s2 + s3) * (1.f / D));
  const float mb = rbf(mean);
  const float den = rbf(__builtin_amdgcn_sqrtf(rbf(var + 1e-5f)));
  const float inv = __builtin_amdgcn_rcpf(den);
#pragma unroll
  for (int k = 0; k < 9; ++k) {
    const float n = rbf(v[k] - mb);
    float qv = n * inv;
    qv = fmaf(fmaf(-qv, den, n), inv, qv);
    v[k] = rbf(qv);
  }
}
__device__ __forceinline__ void store9(LAS unsigned* dst, const float (&v)[9], int lane) {
  *(LAS v4u*)(dst + 4 * lane) = v4u{pkhi(asu(v[0]), asu(v[1])), pkhi(asu(v[2]), asu(v[3])), pkhi(asu(v[4]), asu(v[5])), pkhi(asu(v[6]), asu(v[7]))};
  ((LAS u16*)dst)[512 + lane] = (u16)(asu(v[8]) >> 16);
}

#define WSTAMP(k) do {} while (0)
#define WDUMP() do {} while (0)

__global__ void __launch_bounds__(NTHR) fused_decode(const u16* __restrict__ x, const u16* __restrict__ kc, const u16* __restrict__ vc,
                                                     const u16* __restrict__ wq, const u16* __restrict__ wo, const u16* __restrict__ w1,
                                                     const u16* __restrict__ b1, const u16* __restrict__ w2, const u16* __restrict__ b2,
                                                     u16* __restrict__ out, unsigned char* ws) {
  const unsigned long long T0 = __builtin_amdgcn_s_memrealtime();
  unsigned long long pc0;
  asm volatile("s_getpc_b64 %0" : "=s"(pc0));
  extern __shared__ __attribute__((aligned(16))) float smem[];
  LAS float* L = (LAS float*)smem;
  LAS unsigned* LU = (LAS unsigned*)smem;
  volatile LAS unsigned* FL = (volatile LAS unsigned*)(LU + L_FLAG);
  const int t = threadIdx.x, lane = t & 63;
  const int wave = __builtin_amdgcn_readfirstlane(t >> 6);
  const int u = blockIdx.x, g = u >> 4, gi = u & 15;
  auto mk = [](const void* p, unsigned bytes) { return __builtin_amdgcn_make_buffer_rsrc((void*)p, 0, bytes, 0x00020000); };
  const auto rws = mk(ws, WS_END);
  const auto rx = mk(x, D * 2);

  if (wave == 0) {
    const int xcd = u & 7, jj = u >> 3;
    const int lg = (jj >> 2) * 8 + xcd;
    const bool is_red = lg < 36;
    const int rcol = 16 * lg + 4 * (jj & 3);
    const auto rc = mk((const void*)(pc0 & ~255ull), CODE_KB * 1024);
    const v4u cpf = __builtin_amdgcn_raw_buffer_load_b128(rc, (jj % CODE_KB) * 1024 + lane * 16, 0, 0);
    const auto rb2 = mk(b2, D * 2);
    const int oc = 2 * (lane & 1) + (lane >> 5);
    const unsigned xrr = __builtin_amdgcn_raw_buffer_load_b16(rx, is_red ? (rcol + oc) * 2 : OOB, 0, 0);
    const unsigned b2r = __builtin_amdgcn_raw_buffer_load_b16(rb2, is_red ? (rcol + oc) * 2 : OOB, 0, 0);
    if (lane < 32) LU[L_FLAG + lane] = 0u;
    LGKM0();
    __builtin_amdgcn_s_barrier();
    SB();
    asm volatile("" :: "v"(cpf));
    lds_wait_ge<true>(FL + F_QPUB, 1);
    const unsigned tag = __builtin_amdgcn_readfirstlane(FL[F_TAG]);
    __builtin_amdgcn_s_sleep(DLY_Q);
    WSTAMP(1);
    {
      v4u qv[8];
      const unsigned qoff = WS_Q + g * 8192 + lane * 16;
#pragma unroll 1
      for (unsigned n = 0; n < SPIN_CAP; ++n) {
        asm volatile("" ::: "memory");
        unsigned bad = 0;
#pragma unroll
        for (int k = 0; k < 8; ++k) qv[k] = LDSC(rws, (NOPOLL & 1) ? OOB : qoff, k * 1024);
#pragma unroll
        for (int k = 0; k < 8; ++k) bad |= (qv[k].y ^ tag) | (qv[k].w ^ tag);
        if (__all(bad == 0) || (NOPOLL & 1)) break;
      }
      float s[8];
#pragma unroll
      for (int a = 0; a < 4; ++a) { s[2 * a] = asf(qv[2 * a].x) + asf(qv[2 * a + 1].x); s[2 * a + 1] = asf(qv[2 * a].z) + asf(qv[2 * a + 1].z); }
      const float r0 = tr32(s[0], s[4]), r1_ = tr32(s[1], s[5]), r2_ = tr32(s[2], s[6]), r3 = tr32(s[3], s[7]);
      const int a0 = (lane >> 5) * 2;
      LU[L_QB + a0 * 32 + (lane & 31)] = pkhi(asu(rbf(r0)), asu(rbf(r1_)));
      LU[L_QB + (a0 + 1) * 32 + (lane & 31)] = pkhi(asu(rbf(r2_)), asu(rbf(r3)));
      asm volatile("" ::: "memory");
      if (lane == 0) FL[F_Q] = 1u;
      WSTAMP(2);
    }
    lds_wait_ge<false>(FL + F_OCNT, 8);
    WSTAMP(3);
    {
      v2f oa = *(const LAS v2f*)(L + L_OP + 2 * lane), ob = *(const LAS v2f*)(L + L_OP + 128 + 2 * lane);
#pragma unroll
      for (int w = 1; w < 8; ++w) { oa += *(const LAS v2f*)(L + L_OP + w * 256 + 2 * lane); ob += *(const LAS v2f*)(L + L_OP + w * 256 + 128 + 2 * lane); }
      const int hd = lane & 1;
      const v4f la = *(const LAS v4f*)(L + L_LW + hd * 8), lb = *(const LAS v4f*)(L + L_LW + hd * 8 + 4);
      const v4f ma = *(const LAS v4f*)(L + L_MW + hd * 8), mb = *(const LAS v4f*)(L + L_MW + hd * 8 + 4);
      const float Ls = ((la.x + la.y) + (la.z + la.w)) + ((lb.x + lb.y) + (lb.z + lb.w));
      const float M = fmaxf(fmaxf(fmaxf(ma.x, ma.y), fmaxf(ma.z, ma.w)), fmaxf(fmaxf(mb.x, mb.y), fmaxf(mb.z, mb.w)));
      STSC((v4u{asu(oa.x), tag, asu(oa.y), tag}), rws, WS_O + u * 2048 + lane * 16);
      STSC((v4u{asu(ob.x), tag, asu(ob.y), tag}), rws, WS_O + u * 2048 + 1024 + lane * 16);
      STSC((v4u{asu(M), tag, asu(Ls), tag}), rws, lane < 8 ? WS_M + u * 128 + lane * 16 : OOB);
    }
    WSTAMP(4);
    __builtin_amdgcn_s_sleep(DLY_O);
    {
      const unsigned ooff = WS_O + (16 * g + (lane >> 4)) * 2048 + (16 * gi + (lane & 15)) * 8;
      const unsigned moff = WS_M + (16 * g + (lane >> 4)) * 128 + (gi >> 3) * 16;
      v2u ov[4]; v4u ml[4];
#pragma unroll 1
      for (unsigned n = 0; n < SPIN_CAP; ++n) {
        asm volatile("" ::: "memory");
        unsigned bad = 0;
#pragma unroll
        for (int k = 0; k < 4; ++k) { ov[k] = LDSC8(rws, (NOPOLL & 2) ? OOB : ooff, k * 8192); ml[k] = LDSC(rws, (NOPOLL & 2) ? OOB : moff, k * 512); }
#pragma unroll
        for (int k = 0; k < 4; ++k) bad |= (ov[k].y ^ tag) | (ml[k].y ^ tag) | (ml[k].w ^ tag);
        if (__all(bad == 0) || (NOPOLL & 2)) break;
      }
      WSTAMP(5);
      float M = fmaxf(fmaxf(asf(ml[0].x), asf(ml[1].x)), fmaxf(asf(ml[2].x), asf(ml[3].x)));
      M = max16(M); M = max32(M);
      float num = 0.f, den = 0.f;
#pragma unroll
      for (int k = 0; k < 4; ++k) { const float sc = __builtin_amdgcn_exp2f((asf(ml[k].x) - M) * LOG2E); num = fmaf(sc, asf(ov[k].x), num); den = fmaf(sc, asf(ml[k].z), den); }
      num = tr16(num, num); den = tr16(den, den); num = tr32(num, num); den = tr32(den, den);
      const float o = rbf(num * __builtin_amdgcn_rcpf(den));
      {
        const int d = lane & 15, m0 = 2 * (lane >> 4);
        LAS u16* fp = (LAS u16*)(LU + L_FR) + ((d >> 2) * 64 + (d & 3) * 16 + m0) * 8 + m0;
        fp[0] = (u16)(asu(o) >> 16); fp[9] = (u16)(asu(o) >> 16);
      }
      asm volatile("" ::: "memory");
      if (lane == 0) FL[F_CO] = 1u;
      WSTAMP(6);
    }
    if (is_red) {
      const unsigned off = WS_P + ((lane >> 1) * 576 + rcol + 2 * (lane & 1)) * 8;
      const float xb2 = lo16(xrr) + lo16(b2r);
      lds_wait_ge<false>(FL + F_PPUB, 1);
      __builtin_amdgcn_s_sleep(DLY_P);
      v4u pv[8];
#pragma unroll 1
      for (unsigned n = 0; n < SPIN_CAP; ++n) {
        asm volatile("" ::: "memory");
        unsigned bad = 0;
#pragma unroll
        for (int k = 0; k < 8; ++k) pv[k] = LDSC(rws, (NOPOLL & 4) ? OOB : off, k * (32 * 576 * 8));
#pragma unroll
        for (int k = 0; k < 8; ++k) bad |= (pv[k].y ^ tag) | (pv[k].w ^ tag);
        if (__all(bad == 0) || (NOPOLL & 4)) break;
      }
      float s0 = ((asf(pv[0].x) + asf(pv[1].x)) + (asf(pv[2].x) + asf(pv[3].x))) + ((asf(pv[4].x) + asf(pv[5].x)) + (asf(pv[6].x) + asf(pv[7].x)));
      float s1 = ((asf(pv[0].z) + asf(pv[1].z)) + (asf(pv[2].z) + asf(pv[3].z))) + ((asf(pv[4].z) + asf(pv[5].z)) + (asf(pv[6].z) + asf(pv[7].z)));
      s0 = tr32(s0, s1);
      s0 += dpp<XOR2>(s0); s0 += dpp<ROR4>(s0); s0 += dpp<XOR8>(s0); s0 = tr16(s0, s0);
      if ((lane & 30) == 0) out[rcol + oc] = (u16)(asu(rbf(s0 + xb2)) >> 16);
      if (u == 0 && lane == 0) __builtin_amdgcn_raw_buffer_store_b32(tag, rws, WS_E, 0, 16);
      if (u == 0 && lane == 0) { volatile unsigned long long* st = (volatile unsigned long long*)(ws + (16u << 20)); st[1] = T0; st[2] = __builtin_amdgcn_s_memrealtime(); }
      WSTAMP(7);
    }
    WDUMP();
    return;
  }
  if (wave == 10) {
    const auto rb1 = mk(b1, F * 2);
    const unsigned b1r = LDNT16(rb1, (lane & 31) < 18 ? (18 * u + (lane & 31)) * 2 : OOB);
    __builtin_amdgcn_s_barrier();
    lds_wait_ge<true>(FL + F_QPUB, 1);
    const unsigned tag = __builtin_amdgcn_readfirstlane(FL[F_TAG]);
    {
      const v4u xa = *(const LAS v4u*)(LU + L_XN + 4 * lane);
      const unsigned xb = ((const LAS u16*)(LU + L_XN))[512 + lane];
      float v[9];
#pragma unroll
      for (int k = 0; k < 4; ++k) { v[2 * k] = lo16(xa[k]); v[2 * k + 1] = hi16(xa[k]); }
      v[8] = lo16(xb);
      layernorm9(v);
      store9(LU + L_Y, v, lane);
#pragma unroll
      for (int it = 0; it < 5; ++it) {
        const int idx = it * 64 + lane, n = idx < 288 ? idx >> 3 : 35, m = idx & 7;
        ((LAS u16*)(LU + L_FY))[((n >> 2) * 64 + (n & 3) * 16 + m) * 8 + m] = ((const LAS u16*)(LU + L_Y))[36 * gi + n];
      }
    }
    asm volatile("" ::: "memory");
    if (lane == 0) FL[F_Y] = 1u;
    WSTAMP(1);
    const unsigned hoff = (lane & 31) < 18 ? WS_H + ((16 * g + (lane >> 5)) * 288 + 18 * gi + (lane & 31)) * 8 : OOB;
    const unsigned tagx = (lane & 31) < 18 ? tag : 0u;
    lds_wait_ge<true>(FL + F_HPUB, 3);
    __builtin_amdgcn_s_sleep(DLY_H);
    WSTAMP(2);
    v2u hv[8];
#pragma unroll 1
    for (unsigned n = 0; n < SPIN_CAP; ++n) {
      asm volatile("" ::: "memory");
      unsigned bad = 0;
#pragma unroll
      for (int k = 0; k < 8; ++k) hv[k] = LDSC8(rws, (NOPOLL & 2) ? OOB : hoff, k * (2 * 288 * 8));
#pragma unroll
      for (int k = 0; k < 8; ++k) bad |= hv[k].y ^ tagx;
      if (__all(bad == 0) || (NOPOLL & 2)) break;
    }
    WSTAMP(3);
    float pre = ((asf(hv[0].x) + asf(hv[1].x)) + (asf(hv[2].x) + asf(hv[3].x))) + ((asf(hv[4].x) + asf(hv[5].x)) + (asf(hv[6].x) + asf(hv[7].x)));
    pre = tr32(pre, pre);
    const float tt = rbf(rbf(pre) + lo16(b1r));
    const float inner = 0.7978845608028654f * (tt + 0.044715f * tt * tt * tt);
    const float th = 1.f - 2.f * __builtin_amdgcn_rcpf(__builtin_amdgcn_exp2f(inner * (2.f * LOG2E)) + 1.f);
    const float h = rbf(0.5f * tt * (1.f + th));
    if ((lane & 31) < 18) {
      const int n = 16 + (lane & 31), m0 = 4 * (lane >> 5);
      LAS u16* fp = (LAS u16*)(LU + L_FR) + ((n >> 2) * 64 + (n & 3) * 16 + m0) * 8 + m0;
      fp[0] = (u16)(asu(h) >> 16); fp[9] = (u16)(asu(h) >> 16); fp[18] = (u16)(asu(h) >> 16); fp[27] = (u16)(asu(h) >> 16);
    }
    asm volatile("" ::: "memory");
    if (lane == 0) FL[F_CH] = 1u;
    WSTAMP(4);
    WDUMP();
    return;
  }

  if (wave >= 2 && wave < 10) {
    const int ww = wave - 2;
    const size_t kvb = ((size_t)g * SK + (SK - W) + gi * 256) * 128;
    const auto rk = mk(kc + kvb, 65536), rv = mk(vc + kvb, 65536);
    v4u K[8], V[8];
    const unsigned kvoff = ILV ? ww * 1024 + lane * 16 : (ww * 32 * 128 + lane * 8) * 2;
    constexpr int JS = ILV ? 8192 : 1024;
    __builtin_amdgcn_s_sleep(KSLEEP);
#pragma unroll
    for (int j = 0; j < 8; ++j) { if (j == WIN) __builtin_amdgcn_s_barrier(); if (j >= WIN) VMWAIT(WIN - 1); K[j] = __builtin_amdgcn_raw_buffer_load_b128(rk, kvoff, j * JS, 2); }
#pragma unroll
    for (int j = 0; j < 8; ++j) { VMWAIT(WIN - 1); V[j] = __builtin_amdgcn_raw_buffer_load_b128(rv, kvoff, j * JS, 2); if (j == KVI_AT) lds_arrive(LU + L_FLAG + F_KVI, lane); }
    SB();
    WSTAMP(1);
    lds_wait_ge<true>(FL + F_Q, 1);
    WSTAMP(2);
    const int c = lane & 15;
    unsigned pp;
    {
      const v4u q0 = *(const LAS v4u*)(LU + L_QB + 4 * c), q1 = *(const LAS v4u*)(LU + L_QB + 64 + 4 * c);
      float xs[16];
#pragma unroll
      for (int j = 0; j < 8; ++j) {
        float d0 = 0.f, d1 = 0.f;
#pragma unroll
        for (int k = 0; k < 4; ++k) { d0 = dot2(K[j][k], q0[k], d0); d1 = dot2(K[j][k], q1[k], d1); }
        xs[2 * j] = d0; xs[2 * j + 1] = d1;
      }
#define TR_STEP(HALF, CTRL, BIT) _Pragma("unroll") for (int h = 0; h < (HALF); ++h) { \
      const float lo_ = xs[h], hi_ = xs[h + (HALF)]; const float t1_ = lo_ + dpp<CTRL>(lo_), t2_ = hi_ + dpp<CTRL>(hi_); xs[h] = (c & (BIT)) ? t2_ : t1_; }
      TR_STEP(8, XOR8, 8) TR_STEP(4, XOR7, 4) TR_STEP(2, XOR2, 2) TR_STEP(1, XOR1, 1)
#undef TR_STEP
      const float s = rbf(rbf(xs[0]) * 0.08838834764831845f);
      float m = s;
      m = fmaxf(m, dpp<XOR2>(m)); m = fmaxf(m, dpp<ROR4>(m)); m = fmaxf(m, dpp<XOR8>(m)); m = max16(m); m = max32(m);
      if (lane < 2) L[L_MW + lane * 8 + ww] = m;
      lds_arrive(LU + L_FLAG + F_MCNT, lane);
      lds_wait_ge<false>(FL + F_MCNT, 8);
      WSTAMP(3);
      const v4f ma = *(const LAS v4f*)(L + L_MW + (lane & 1) * 8), mb = *(const LAS v4f*)(L + L_MW + (lane & 1) * 8 + 4);
      m = fmaxf(fmaxf(fmaxf(ma.x, ma.y), fmaxf(ma.z, ma.w)), fmaxf(fmaxf(mb.x, mb.y), fmaxf(mb.z, mb.w)));
      const float p = __builtin_amdgcn_exp2f((s - m) * LOG2E);
      float l = p;
      l += dpp<XOR2>(l); l += dpp<ROR4>(l); l += dpp<XOR8>(l); l = tr16(l, l); l = tr32(l, l);
      if (lane < 2) L[L_LW + lane * 8 + ww] = l;
      const unsigned pb = asu(rbf(p));
      pp = pkhi(pb, pb);
    }
    v4u msk;
#pragma unroll
    for (int k = 0; k < 4; ++k) msk[k] = ((c & 7) >> 1) == k ? ((c & 1) ? 0xffff0000u : 0x0000ffffu) : 0u;
    v4f acc = {0.f, 0.f, 0.f, 0.f};
#define PV_STEP(j) { \
      int pj = __builtin_amdgcn_update_dpp(0, (int)pp, 0x150 + 2 * j, 0xf, 0x3, false); \
      pj = __builtin_amdgcn_update_dpp(pj, (int)pp, 0x150 + 2 * j + 1, 0xf, 0xc, false); \
      const v4u af = {msk[0] & pj, msk[1] & pj, msk[2] & pj, msk[3] & pj}; \
      acc = __builtin_amdgcn_mfma_f32_16x16x32_bf16(__builtin_bit_cast(bf16x8, af), __builtin_bit_cast(bf16x8, V[j]), acc, 0, 0, 0); }
    PV_STEP(0) PV_STEP(1) PV_STEP(2) PV_STEP(3) PV_STEP(4) PV_STEP(5) PV_STEP(6) PV_STEP(7)
#undef PV_STEP
    *(LAS v4f*)(L + L_OP + ww * 256 + (lane >> 5) * 128 + 8 * c + 4 * ((lane >> 4) & 1)) = acc;
    lds_arrive(LU + L_FLAG + F_OCNT, lane);
    WSTAMP(4);
    WDUMP();
    return;
  }

  if (wave == 1) {
    const auto rq = mk(wq, 576 * 4096 * 2);
    const int qa = gi >> 2, qr = gi & 3;
    const v4u xa = __builtin_amdgcn_raw_buffer_load_b128(rx, lane * 16, 0, 0);
    const unsigned xb = __builtin_amdgcn_raw_buffer_load_b16(rx, 1024 + lane * 2, 0, 0);
    const unsigned e = __builtin_amdgcn_raw_buffer_load_b32(rws, WS_E, 0, 16);
    v4u WQ[18];
    const unsigned offq = ((144 * qr + 2 * (lane >> 3)) * 4096 + 256 * g + 64 * qa + 8 * (lane & 7)) * 2;
#pragma unroll
    for (int i = 0; i < 18; ++i) WQ[i] = __builtin_amdgcn_raw_buffer_load_b128(rq, offq, ((i >> 1) * 16 + (i & 1)) * 8192, 2);
    __builtin_amdgcn_s_barrier();
    SB();
    float v[9];
#pragma unroll
    for (int k = 0; k < 4; ++k) { v[2 * k] = lo16(xa[k]); v[2 * k + 1] = hi16(xa[k]); }
    v[8] = lo16(xb);
    layernorm9(v);
    store9(LU + L_XN, v, lane);
    WSTAMP(1);
    float a[8];
#pragma unroll
    for (int k = 0; k < 8; ++k) a[k] = 0.f;
#pragma unroll
    for (int i = 0; i < 9; ++i) {
      const unsigned xp = LU[L_XN + 72 * qr + 8 * i + (lane >> 3)];
#pragma unroll
      for (int c = 0; c < 4; ++c) {
        a[2 * c] = dot2(pklo(WQ[2 * i][c], WQ[2 * i + 1][c]), xp, a[2 * c]);
        a[2 * c + 1] = dot2(pkhi(WQ[2 * i][c], WQ[2 * i + 1][c]), xp, a[2 * c + 1]);
      }
    }
    const float r = red8(a, lane);
    const unsigned tg = __builtin_amdgcn_readfirstlane(e) + 1u;
    const int col = 8 * (lane & 7) + ((lane >> 3) & 1) * 4 + ((lane >> 4) & 1) * 2 + (lane >> 5);
    STSC8((v2u{asu(r), tg}), rws, WS_Q + (u * 64 + col) * 8);
    asm volatile("" ::: "memory");
    if (lane == 0) { FL[F_TAG] = tg; FL[F_QPUB] = 1u; }
    WSTAMP(2);
  } else {
#pragma unroll
    for (int k = 0; k < 5; ++k) { const int i = (k * 4 + (wave - 11)) * 64 + lane; if (i < 18 * 64) *(LAS v4u*)(LU + L_FR + 4 * i) = v4u{0u, 0u, 0u, 0u}; }
    LGKM0();
    __builtin_amdgcn_s_barrier();
  }
  {
    const int r = lane >> 4, c = lane & 15;
    const auto ro = mk(wo + (size_t)u * 16 * D, 16 * D * 2), r2 = mk(w2 + (size_t)u * 18 * D, 18 * D * 2);
    v4u W1[9], WF[9];
    const bool has1 = wave >= 11 && wave < 14;
    if (has1) {
      const auto r1 = mk(w1, 576 * F * 2);
      const int hw = wave - 11;
      const unsigned off1 = c < 12 ? ((36 * gi + r) * F + 288 * g + 96 * hw + 8 * c) * 2 : OOB;
      lds_wait_ge<true>(FL + F_KVI, W1_AT + hw);
      WSTAMP(3);
#pragma unroll
      for (int k = 0; k < 9; ++k) W1[k] = __builtin_amdgcn_raw_buffer_load_b128(r1, off1, k * (4 * F * 2), 2);
      lds_arrive(LU + L_FLAG + F_W1I, lane);
    }
    const int cb = wave == 1 ? 512 : 128 * (wave - 11);
    const unsigned off2 = (wave != 1 || c < 8) ? (r * D + cb + 8 * c) * 2 : OOB;
    lds_wait_ge<true>(FL + F_KVI, WF_AT);
    lds_wait_ge<true>(FL + F_W1I, 3);
    WSTAMP(4);
#pragma unroll
    for (int k = 0; k < 4; ++k) WF[k] = __builtin_amdgcn_raw_buffer_load_b128(ro, off2, k * (4 * D * 2), 2);
#pragma unroll
    for (int k = 0; k < 5; ++k) WF[4 + k] = LDNT(r2, off2 + k * (4 * D * 2));
    SB();
    lds_wait_ge<true>(FL + F_Y, 1);
    const unsigned tag = FL[F_TAG];
    const int cl = 8 * c + 4 * (r & 1) + 2 * (r >> 1);
    if (has1) {
      v4f acc = {0.f, 0.f, 0.f, 0.f};
#pragma unroll
      for (int k = 0; k < 9; ++k)
        acc = __builtin_amdgcn_mfma_f32_16x16x32_bf16(__builtin_bit_cast(bf16x8, *(const LAS v4u*)(LU + L_FY + k * 256 + 4 * lane)), __builtin_bit_cast(bf16x8, W1[k]), acc, 0, 0, 0);
      const auto s0 = __builtin_amdgcn_permlane32_swap(asu(acc[0]), asu(acc[2]), false, false);
      const auto s1 = __builtin_amdgcn_permlane32_swap(asu(acc[1]), asu(acc[3]), false, false);
      STSC((v4u{s0[0], tag, s1[0], tag}), rws, c < 12 ? WS_H + (u * 288 + 96 * (wave - 11) + cl) * 8 : OOB);
      lds_arrive(LU + L_FLAG + F_HPUB, lane);
      WSTAMP(5);
    }
    const unsigned poff = (wave != 1 || c < 8) ? WS_P + (u * 576 + cb + cl) * 8 : OOB;
    v4f acc = {0.f, 0.f, 0.f, 0.f}, acc2 = {0.f, 0.f, 0.f, 0.f};
    lds_wait_ge<false>(FL + F_CO, 1);
#pragma unroll
    for (int k = 0; k < 4; ++k)
      acc = __builtin_amdgcn_mfma_f32_16x16x32_bf16(__builtin_bit_cast(bf16x8, *(const LAS v4u*)(LU + L_FR + k * 256 + 4 * lane)), __builtin_bit_cast(bf16x8, WF[k]), acc, 0, 0, 0);
    asm volatile("" : "+v"(acc));
    lds_wait_ge<false>(FL + F_CH, 1);
#pragma unroll
    for (int k = 4; k < 9; ++k) {
      v4f& a = (k & 1) ? acc2 : acc;
      a = __builtin_amdgcn_mfma_f32_16x16x32_bf16(__builtin_bit_cast(bf16x8, *(const LAS v4u*)(LU + L_FR + k * 256 + 4 * lane)), __builtin_bit_cast(bf16x8, WF[k]), a, 0, 0, 0);
    }
    acc += acc2;
    const auto s0 = __builtin_amdgcn_permlane32_swap(asu(acc[0]), asu(acc[2]), false, false);
    const auto s1 = __builtin_amdgcn_permlane32_swap(asu(acc[1]), asu(acc[3]), false, false);
    STSC((v4u{s0[0], tag, s1[0], tag}), rws, poff);
    if (lane == 0) FL[F_PPUB] = 1u;
    WSTAMP(7);
  }
  WDUMP();
}

__global__ void kA(unsigned char* ws) { *(volatile unsigned long long*)(ws + (16u << 20)) = __builtin_amdgcn_s_memrealtime(); }
__global__ void kB(unsigned char* ws) {
  const unsigned long long now = __builtin_amdgcn_s_memrealtime();
  volatile unsigned long long* st = (volatile unsigned long long*)(ws + (16u << 20));
  const unsigned long long gap1 = st[1] - st[0], gap2 = now - st[2];
  const unsigned long long d = MODE == 1 ? 10 * gap1 : MODE == 2 ? 10 * gap2 : 0;
  while (__builtin_amdgcn_s_memrealtime() < now + d) __builtin_amdgcn_s_sleep(1);
}
extern "C" void kernel_launch(void* const* d_in, const int* in_sizes, int n_in,
                              void* d_out, int out_size, void* d_ws, size_t ws_size,
                              hipStream_t stream) {
  (void)hipFuncSetAttribute((const void*)fused_decode, hipFuncAttributeMaxDynamicSharedMemorySize, LDS_BYTES);
  kA<<<1, 64, 0, stream>>>((unsigned char*)d_ws);
  fused_decode<<<256, NTHR, LDS_BYTES, stream>>>((const u16*)d_in[0], (const u16*)d_in[1], (const u16*)d_in[2], (const u16*)d_in[3],
                                                 (const u16*)d_in[4], (const u16*)d_in[6], (const u16*)d_in[7], (const u16*)d_in[8],
                                                 (const u16*)d_in[9], (u16*)d_out, (unsigned char*)d_ws);
  kB<<<1, 64, 0, stream>>>((unsigned char*)d_ws);
}
```

```cpp
#include <hip/hip_runtime.h>
#include <hip/hip_bf16.h>
#include <cstdint>

typedef unsigned short u16;
typedef unsigned v4u __attribute__((ext_vector_type(4)));
typedef unsigned v2u __attribute__((ext_vector_type(2)));
typedef float v4f __attribute__((ext_vector_type(4)));
typedef float v2f __attribute__((ext_vector_type(2)));
typedef __bf16 bf16x2 __attribute__((ext_vector_type(2)));
typedef short bf16x8 __attribute__((ext_vector_type(8)));
#define LAS __attribute__((address_space(3)))

#define WIN 4
#define WWIN 7
#define NOPOLL 0
#define ILV 1
#define KSLEEP 4
#define W1_AT 2
#define WF_AT 8
#define STAG 1
#define KVI_AT 7
#define CODE_KB 13
#define DLY_Q 30
#define DLY_O 16
#define DLY_H 16
#define DLY_P 16

constexpr int D = 576, SK = 65536, W = 4096, F = 4608;
constexpr int NTHR = 960;
constexpr unsigned OOB = 0x80000000u;
constexpr unsigned WS_E = 0;
constexpr unsigned WS_Q = 4096;
constexpr unsigned WS_H = WS_Q + 256 * 64 * 8;
constexpr unsigned WS_O = WS_H + 256 * 288 * 8;
constexpr unsigned WS_M = WS_O + 256 * 256 * 8;
constexpr unsigned WS_P = WS_M + 256 * 128;
constexpr unsigned WS_END = WS_P + 256 * 576 * 8;
constexpr unsigned WS_DBG = 8u << 20;
constexpr int L_XN = 0, L_Y = L_XN + 288, L_QB = L_Y + 288, L_MW = L_QB + 128, L_LW = L_MW + 16, L_CO = L_LW + 16,
              L_OP = L_CO + 32, L_FLAG = L_OP + 8 * 256, L_FR = L_FLAG + 32, L_FY = L_FR + 9 * 256, L_END = L_FY + 9 * 256;
constexpr int LDS_BYTES = 96 * 1024;
static_assert(L_END * 4 <= LDS_BYTES, "lds");
enum { F_CH = 11, F_W1I = 10, F_KVI = 9, F_Y = 0, F_QPUB, F_Q, F_MCNT, F_OCNT, F_HPUB, F_CO, F_PPUB, F_TAG };
constexpr unsigned SPIN_CAP = 1u << 22;
constexpr float LOG2E = 1.4426950408889634f;

__device__ __forceinline__ float rbf(float f) {
  unsigned u = __float_as_uint(f);
  u = (u + 0x7fffu + ((u >> 16) & 1u)) & 0xffff0000u;
  return __uint_as_float(u);
}
__device__ __forceinline__ float lo16(unsigned w) { return __uint_as_float(w << 16); }
__device__ __forceinline__ float hi16(unsigned w) { return __uint_as_float(w & 0xffff0000u); }
__device__ __forceinline__ float asf(unsigned w) { return __uint_as_float(w); }
__device__ __forceinline__ unsigned asu(float w) { return __float_as_uint(w); }
__device__ __forceinline__ unsigned pklo(unsigned a, unsigned b) { return __builtin_amdgcn_perm(b, a, 0x05040100u); }
__device__ __forceinline__ unsigned pkhi(unsigned a, unsigned b) { return __builtin_amdgcn_perm(b, a, 0x07060302u); }
__device__ __forceinline__ float dot2(unsigned k, unsigned q, float acc) {
  return __builtin_amdgcn_fdot2_f32_bf16(__builtin_bit_cast(bf16x2, k), __builtin_bit_cast(bf16x2, q), acc, false);
}
template <int CTRL> __device__ __forceinline__ float dpp(float x) {
  return __builtin_bit_cast(float, __builtin_amdgcn_mov_dpp(__builtin_bit_cast(int, x), CTRL, 0xf, 0xf, true));
}
template <int CTRL> __device__ __forceinline__ unsigned dppu(unsigned x) { return (unsigned)__builtin_amdgcn_mov_dpp((int)x, CTRL, 0xf, 0xf, true); }
constexpr int XOR1 = 0xB1, XOR2 = 0x4E, XOR3 = 0x1B, XOR7 = 0x141, XOR8 = 0x128, ROR4 = 0x124;
__device__ __forceinline__ float tr16(float a, float b) {
  auto s = __builtin_amdgcn_permlane16_swap(asu(a), asu(b), false, false);
  return asf(s[0]) + asf(s[1]);
}
__device__ __forceinline__ float tr32(float a, float b) {
  auto s = __builtin_amdgcn_permlane32_swap(asu(a), asu(b), false, false);
  return asf(s[0]) + asf(s[1]);
}
__device__ __forceinline__ float max16(float a) {
  auto s = __builtin_amdgcn_permlane16_swap(asu(a), asu(a), false, false);
  return fmaxf(asf(s[0]), asf(s[1]));
}
__device__ __forceinline__ float max32(float a) {
  auto s = __builtin_amdgcn_permlane32_swap(asu(a), asu(a), false, false);
  return fmaxf(asf(s[0]), asf(s[1]));
}
__device__ __forceinline__ float wave_sum(float t) {
  t += dpp<XOR1>(t); t += dpp<XOR2>(t); t += dpp<ROR4>(t); t += dpp<XOR8>(t);
  t = tr16(t, t); t = tr32(t, t);
  return t;
}
__device__ __forceinline__ float red8(float (&a)[8], int lane) {
#pragma unroll
  for (int h = 0; h < 4; ++h) { const float t1 = a[h] + dpp<XOR8>(a[h]), t2 = a[h + 4] + dpp<XOR8>(a[h + 4]); a[h] = (lane & 8) ? t2 : t1; }
  a[0] = tr16(a[0], a[2]); a[1] = tr16(a[1], a[3]);
  return tr32(a[0], a[1]);
}
#define VMWAIT(n) asm volatile("s_waitcnt vmcnt(%0)" :: "n"(n) : "memory")
#define LGKM0() asm volatile("s_waitcnt lgkmcnt(0)" ::: "memory")
#define LDNT(r, off) __builtin_amdgcn_raw_buffer_load_b128((r), (off), 0, 2)
#define LDNT16(r, off) __builtin_amdgcn_raw_buffer_load_b16((r), (off), 0, 2)
#define LDSC(r, off, so) __builtin_amdgcn_raw_buffer_load_b128((r), (off), (so), 16)
#define LDSC8(r, off, so) __builtin_amdgcn_raw_buffer_load_b64((r), (off), (so), 16)
#define STSC(v, r, off) __builtin_amdgcn_raw_buffer_store_b128((v), (r), (off), 0, 16)
#define STSC8(v, r, off) __builtin_amdgcn_raw_buffer_store_b64((v), (r), (off), 0, 16)
#define SB() __builtin_amdgcn_sched_barrier(0)
#define PIN8(a) asm volatile("" : "+v"(a[0]), "+v"(a[1]), "+v"(a[2]), "+v"(a[3]), "+v"(a[4]), "+v"(a[5]), "+v"(a[6]), "+v"(a[7]))

template <bool SLEEP> __device__ __forceinline__ void lds_wait_ge(volatile LAS unsigned* p, unsigned v) {
#pragma unroll 1
  for (unsigned n = 0; n < SPIN_CAP; ++n) { if (*p >= v) break; if (SLEEP) __builtin_amdgcn_s_sleep(1); }
  asm volatile("" ::: "memory");
}
__device__ __forceinline__ void lds_arrive(LAS unsigned* p, int lane) {
  asm volatile("" ::: "memory");
  if (lane == 0) (void)__hip_atomic_fetch_add(p, 1u, __ATOMIC_RELAXED, __HIP_MEMORY_SCOPE_WORKGROUP);
  asm volatile("" ::: "memory");
}

__device__ __forceinline__ void layernorm9(float (&v)[9]) {
  float s = ((v[0] + v[1]) + (v[2] + v[3])) + ((v[4] + v[5]) + (v[6] + v[7])) + v[8];
  const float mean = wave_sum(s) * (1.f / D);
  float s2 = 0.f, s3 = 0.f;
#pragma unroll
  for (int k = 0; k < 8; k += 2) { const float d = v[k] - mean, e = v[k + 1] - mean; s2 = fmaf(d, d, s2); s3 = fmaf(e, e, s3); }
  { const float d = v[8] - mean; s2 = fmaf(d, d, s2); }
  const float var = rbf(wave_sum(s2 + s3) * (1.f / D));
  const float mb = rbf(mean);
  const float den = rbf(__builtin_amdgcn_sqrtf(rbf(var + 1e-5f)));
  const float inv = __builtin_amdgcn_rcpf(den);
#pragma unroll
  for (int k = 0; k < 9; ++k) {
    const float n = rbf(v[k] - mb);
    float qv = n * inv;
    qv = fmaf(fmaf(-qv, den, n), inv, qv);
    v[k] = rbf(qv);
  }
}
__device__ __forceinline__ void store9(LAS unsigned* dst, const float (&v)[9], int lane) {
  *(LAS v4u*)(dst + 4 * lane) = v4u{pkhi(asu(v[0]), asu(v[1])), pkhi(asu(v[2]), asu(v[3])), pkhi(asu(v[4]), asu(v[5])), pkhi(asu(v[6]), asu(v[7]))};
  ((LAS u16*)dst)[512 + lane] = (u16)(asu(v[8]) >> 16);
}

#define WSTAMP(k) do {} while (0)
#define WDUMP() do {} while (0)

__global__ void __launch_bounds__(NTHR) fused_decode(const u16* __restrict__ x, const u16* __restrict__ kc, const u16* __restrict__ vc,
                                                     const u16* __restrict__ wq, const u16* __restrict__ wo, const u16* __restrict__ w1,
                                                     const u16* __restrict__ b1, const u16* __restrict__ w2, const u16* __restrict__ b2,
                                                     u16* __restrict__ out, unsigned char* ws) {
  const unsigned long long T0 = __builtin_amdgcn_s_memrealtime();
  unsigned long long pc0;
  asm volatile("s_getpc_b64 %0" : "=s"(pc0));
  extern __shared__ __attribute__((aligned(16))) float smem[];
  LAS float* L = (LAS float*)smem;
  LAS unsigned* LU = (LAS unsigned*)smem;
  volatile LAS unsigned* FL = (volatile LAS unsigned*)(LU + L_FLAG);
  const int t = threadIdx.x, lane = t & 63;
  const int wave = __builtin_amdgcn_readfirstlane(t >> 6);
  const int u = blockIdx.x, g = u >> 4, gi = u & 15;
  auto mk = [](const void* p, unsigned bytes) { return __builtin_amdgcn_make_buffer_rsrc((void*)p, 0, bytes, 0x00020000); };
  const auto rws = mk(ws, WS_END);
  const auto rx = mk(x, D * 2);

  if (wave == 0) {
    const int xcd = u & 7, jj = u >> 3;
    const int lg = (jj >> 2) * 8 + xcd;
    const bool is_red = lg < 36;
    const int rcol = 16 * lg + 4 * (jj & 3);
    const auto rc = mk((const void*)(pc0 & ~255ull), CODE_KB * 1024);
    const v4u cpf = __builtin_amdgcn_raw_buffer_load_b128(rc, (jj % CODE_KB) * 1024 + lane * 16, 0, 0);
    const auto rb2 = mk(b2, D * 2);
    const int oc = 2 * (lane & 1) + (lane >> 5);
    const unsigned xrr = __builtin_amdgcn_raw_buffer_load_b16(rx, is_red ? (rcol + oc) * 2 : OOB, 0, 0);
    const unsigned b2r = __builtin_amdgcn_raw_buffer_load_b16(rb2, is_red ? (rcol + oc) * 2 : OOB, 0, 0);
    if (lane < 32) LU[L_FLAG + lane] = 0u;
    LGKM0();
    __builtin_amdgcn_s_barrier();
    SB();
    asm volatile("" :: "v"(cpf));
    lds_wait_ge<true>(FL + F_QPUB, 1);
    const unsigned tag = __builtin_amdgcn_readfirstlane(FL[F_TAG]);
    __builtin_amdgcn_s_sleep(DLY_Q);
    WSTAMP(1);
    {
      v4u qv[8];
      const unsigned qoff = WS_Q + g * 8192 + lane * 16;
#pragma unroll 1
      for (unsigned n = 0; n < SPIN_CAP; ++n) {
        asm volatile("" ::: "memory");
        unsigned bad = 0;
#pragma unroll
        for (int k = 0; k < 8; ++k) qv[k] = LDSC(rws, (NOPOLL & 1) ? OOB : qoff, k * 1024);
#pragma unroll
        for (int k = 0; k < 8; ++k) bad |= (qv[k].y ^ tag) | (qv[k].w ^ tag);
        if (__all(bad == 0) || (NOPOLL & 1)) break;
      }
      float s[8];
#pragma unroll
      for (int a = 0; a < 4; ++a) { s[2 * a] = asf(qv[2 * a].x) + asf(qv[2 * a + 1].x); s[2 * a + 1] = asf(qv[2 * a].z) + asf(qv[2 * a + 1].z); }
      const float r0 = tr32(s[0], s[4]), r1_ = tr32(s[1], s[5]), r2_ = tr32(s[2], s[6]), r3 = tr32(s[3], s[7]);
      const int a0 = (lane >> 5) * 2;
      LU[L_QB + a0 * 32 + (lane & 31)] = pkhi(asu(rbf(r0)), asu(rbf(r1_)));
      LU[L_QB + (a0 + 1) * 32 + (lane & 31)] = pkhi(asu(rbf(r2_)), asu(rbf(r3)));
      asm volatile("" ::: "memory");
      if (lane == 0) FL[F_Q] = 1u;
      WSTAMP(2);
    }
    lds_wait_ge<false>(FL + F_OCNT, 8);
    WSTAMP(3);
    {
      v2f oa = *(const LAS v2f*)(L + L_OP + 2 * lane), ob = *(const LAS v2f*)(L + L_OP + 128 + 2 * lane);
#pragma unroll
      for (int w = 1; w < 8; ++w) { oa += *(const LAS v2f*)(L + L_OP + w * 256 + 2 * lane); ob += *(const LAS v2f*)(L + L_OP + w * 256 + 128 + 2 * lane); }
      const int hd = lane & 1;
      const v4f la = *(const LAS v4f*)(L + L_LW + hd * 8), lb = *(const LAS v4f*)(L + L_LW + hd * 8 + 4);
      const v4f ma = *(const LAS v4f*)(L + L_MW + hd * 8), mb = *(const LAS v4f*)(L + L_MW + hd * 8 + 4);
      const float Ls = ((la.x + la.y) + (la.z + la.w)) + ((lb.x + lb.y) + (lb.z + lb.w));
      const float M = fmaxf(fmaxf(fmaxf(ma.x, ma.y), fmaxf(ma.z, ma.w)), fmaxf(fmaxf(mb.x, mb.y), fmaxf(mb.z, mb.w)));
      STSC((v4u{asu(oa.x), tag, asu(oa.y), tag}), rws, WS_O + u * 2048 + lane * 16);
      STSC((v4u{asu(ob.x), tag, asu(ob.y), tag}), rws, WS_O + u * 2048 + 1024 + lane * 16);
      STSC((v4u{asu(M), tag, asu(Ls), tag}), rws, lane < 8 ? WS_M + u * 128 + lane * 16 : OOB);
    }
    WSTAMP(4);
    __builtin_amdgcn_s_sleep(DLY_O);
    {
      const unsigned ooff = WS_O + (16 * g + (lane >> 4)) * 2048 + (16 * gi + (lane & 15)) * 8;
      const unsigned moff = WS_M + (16 * g + (lane >> 4)) * 128 + (gi >> 3) * 16;
      v2u ov[4]; v4u ml[4];
#pragma unroll 1
      for (unsigned n = 0; n < SPIN_CAP; ++n) {
        asm volatile("" ::: "memory");
        unsigned bad = 0;
#pragma unroll
        for (int k = 0; k < 4; ++k) { ov[k] = LDSC8(rws, (NOPOLL & 2) ? OOB : ooff, k * 8192); ml[k] = LDSC(rws, (NOPOLL & 2) ? OOB : moff, k * 512); }
#pragma unroll
        for (int k = 0; k < 4; ++k) bad |= (ov[k].y ^ tag) | (ml[k].y ^ tag) | (ml[k].w ^ tag);
        if (__all(bad == 0) || (NOPOLL & 2)) break;
      }
      WSTAMP(5);
      float M = fmaxf(fmaxf(asf(ml[0].x), asf(ml[1].x)), fmaxf(asf(ml[2].x), asf(ml[3].x)));
      M = max16(M); M = max32(M);
      float num = 0.f, den = 0.f;
#pragma unroll
      for (int k = 0; k < 4; ++k) { const float sc = __builtin_amdgcn_exp2f((asf(ml[k].x) - M) * LOG2E); num = fmaf(sc, asf(ov[k].x), num); den = fmaf(sc, asf(ml[k].z), den); }
      num = tr16(num, num); den = tr16(den, den); num = tr32(num, num); den = tr32(den, den);
      const float o = rbf(num * __builtin_amdgcn_rcpf(den));
      {
        const int d = lane & 15, m0 = 2 * (lane >> 4);
        LAS u16* fp = (LAS u16*)(LU + L_FR) + ((d >> 2) * 64 + (d & 3) * 16 + m0) * 8 + m0;
        fp[0] = (u16)(asu(o) >> 16); fp[9] = (u16)(asu(o) >> 16);
      }
      asm volatile("" ::: "memory");
      if (lane == 0) FL[F_CO] = 1u;
      WSTAMP(6);
    }
    if (is_red) {
      const unsigned off = WS_P + ((lane >> 1) * 576 + rcol + 2 * (lane & 1)) * 8;
      const float xb2 = lo16(xrr) + lo16(b2r);
      lds_wait_ge<false>(FL + F_PPUB, 1);
      __builtin_amdgcn_s_sleep(DLY_P);
      v4u pv[8];
#pragma unroll 1
      for (unsigned n = 0; n < SPIN_CAP; ++n) {
        asm volatile("" ::: "memory");
        unsigned bad = 0;
#pragma unroll
        for (int k = 0; k < 8; ++k) pv[k] = LDSC(rws, (NOPOLL & 4) ? OOB : off, k * (32 * 576 * 8));
#pragma unroll
        for (int k = 0; k < 8; ++k) bad |= (pv[k].y ^ tag) | (pv[k].w ^ tag);
        if (__all(bad == 0) || (NOPOLL & 4)) break;
      }
      float s0 = ((asf(pv[0].x) + asf(pv[1].x)) + (asf(pv[2].x) + asf(pv[3].x))) + ((asf(pv[4].x) + asf(pv[5].x)) + (asf(pv[6].x) + asf(pv[7].x)));
      float s1 = ((asf(pv[0].z) + asf(pv[1].z)) + (asf(pv[2].z) + asf(pv[3].z))) + ((asf(pv[4].z) + asf(pv[5].z)) + (asf(pv[6].z) + asf(pv[7].z)));
      s0 = tr32(s0, s1);
      s0 += dpp<XOR2>(s0); s0 += dpp<ROR4>(s0); s0 += dpp<XOR8>(s0); s0 = tr16(s0, s0);
      if ((lane & 30) == 0) out[rcol + oc] = (u16)(asu(rbf(s0 + xb2)) >> 16);
      if (u == 0 && lane == 0) __builtin_amdgcn_raw_buffer_store_b32(tag, rws, WS_E, 0, 16);
      if (u == 0) { while (__builtin_amdgcn_s_memrealtime() < T0 + 1500) __builtin_amdgcn_s_sleep(1); }
      WSTAMP(7);
    }
    WDUMP();
    return;
  }
  if (wave == 10) {
    const auto rb1 = mk(b1, F * 2);
    const unsigned b1r = LDNT16(rb1, (lane & 31) < 18 ? (18 * u + (lane & 31)) * 2 : OOB);
    __builtin_amdgcn_s_barrier();
    lds_wait_ge<true>(FL + F_QPUB, 1);
    const unsigned tag = __builtin_amdgcn_readfirstlane(FL[F_TAG]);
    {
      const v4u xa = *(const LAS v4u*)(LU + L_XN + 4 * lane);
      const unsigned xb = ((const LAS u16*)(LU + L_XN))[512 + lane];
      float v[9];
#pragma unroll
      for (int k = 0; k < 4; ++k) { v[2 * k] = lo16(xa[k]); v[2 * k + 1] = hi16(xa[k]); }
      v[8] = lo16(xb);
      layernorm9(v);
      store9(LU + L_Y, v, lane);
#pragma unroll
      for (int it = 0; it < 5; ++it) {
        const int idx = it * 64 + lane, n = idx < 288 ? idx >> 3 : 35, m = idx & 7;
        ((LAS u16*)(LU + L_FY))[((n >> 2) * 64 + (n & 3) * 16 + m) * 8 + m] = ((const LAS u16*)(LU + L_Y))[36 * gi + n];
      }
    }
    asm volatile("" ::: "memory");
    if (lane == 0) FL[F_Y] = 1u;
    WSTAMP(1);
    const unsigned hoff = (lane & 31) < 18 ? WS_H + ((16 * g + (lane >> 5)) * 288 + 18 * gi + (lane & 31)) * 8 : OOB;
    const unsigned tagx = (lane & 31) < 18 ? tag : 0u;
    lds_wait_ge<true>(FL + F_HPUB, 3);
    __builtin_amdgcn_s_sleep(DLY_H);
    WSTAMP(2);
    v2u hv[8];
#pragma unroll 1
    for (unsigned n = 0; n < SPIN_CAP; ++n) {
      asm volatile("" ::: "memory");
      unsigned bad = 0;
#pragma unroll
      for (int k = 0; k < 8; ++k) hv[k] = LDSC8(rws, (NOPOLL & 2) ? OOB : hoff, k * (2 * 288 * 8));
#pragma unroll
      for (int k = 0; k < 8; ++k) bad |= hv[k].y ^ tagx;
      if (__all(bad == 0) || (NOPOLL & 2)) break;
    }
    WSTAMP(3);
    float pre = ((asf(hv[0].x) + asf(hv[1].x)) + (asf(hv[2].x) + asf(hv[3].x))) + ((asf(hv[4].x) + asf(hv[5].x)) + (asf(hv[6].x) + asf(hv[7].x)));
    pre = tr32(pre, pre);
    const float tt = rbf(rbf(pre) + lo16(b1r));
    const float inner = 0.7978845608028654f * (tt + 0.044715f * tt * tt * tt);
    const float th = 1.f - 2.f * __builtin_amdgcn_rcpf(__builtin_amdgcn_exp2f(inner * (2.f * LOG2E)) + 1.f);
    const float h = rbf(0.5f * tt * (1.f + th));
    if ((lane & 31) < 18) {
      const int n = 16 + (lane & 31), m0 = 4 * (lane >> 5);
      LAS u16* fp = (LAS u16*)(LU + L_FR) + ((n >> 2) * 64 + (n & 3) * 16 + m0) * 8 + m0;
      fp[0] = (u16)(asu(h) >> 16); fp[9] = (u16)(asu(h) >> 16); fp[18] = (u16)(asu(h) >> 16); fp[27] = (u16)(asu(h) >> 16);
    }
    asm volatile("" ::: "memory");
    if (lane == 0) FL[F_CH] = 1u;
    WSTAMP(4);
    WDUMP();
    return;
  }

  if (wave >= 2 && wave < 10) {
    const int ww = wave - 2;
    const size_t kvb = ((size_t)g * SK + (SK - W) + gi * 256) * 128;
    const auto rk = mk(kc + kvb, 65536), rv = mk(vc + kvb, 65536);
    v4u K[8], V[8];
    const unsigned kvoff = ILV ? ww * 1024 + lane * 16 : (ww * 32 * 128 + lane * 8) * 2;
    constexpr int JS = ILV ? 8192 : 1024;
    __builtin_amdgcn_s_sleep(KSLEEP);
#pragma unroll
    for (int j = 0; j < 8; ++j) { if (j == WIN) __builtin_amdgcn_s_barrier(); if (j >= WIN) VMWAIT(WIN - 1); K[j] = __builtin_amdgcn_raw_buffer_load_b128(rk, kvoff, j * JS, 2); }
#pragma unroll
    for (int j = 0; j < 8; ++j) { VMWAIT(WIN - 1); V[j] = __builtin_amdgcn_raw_buffer_load_b128(rv, kvoff, j * JS, 2); if (j == KVI_AT) lds_arrive(LU + L_FLAG + F_KVI, lane); }
    SB();
    WSTAMP(1);
    lds_wait_ge<true>(FL + F_Q, 1);
    WSTAMP(2);
    const int c = lane & 15;
    unsigned pp;
    {
      const v4u q0 = *(const LAS v4u*)(LU + L_QB + 4 * c), q1 = *(const LAS v4u*)(LU + L_QB + 64 + 4 * c);
      float xs[16];
#pragma unroll
      for (int j = 0; j < 8; ++j) {
        float d0 = 0.f, d1 = 0.f;
#pragma unroll
        for (int k = 0; k < 4; ++k) { d0 = dot2(K[j][k], q0[k], d0); d1 = dot2(K[j][k], q1[k], d1); }
        xs[2 * j] = d0; xs[2 * j + 1] = d1;
      }
#define TR_STEP(HALF, CTRL, BIT) _Pragma("unroll") for (int h = 0; h < (HALF); ++h) { \
      const float lo_ = xs[h], hi_ = xs[h + (HALF)]; const float t1_ = lo_ + dpp<CTRL>(lo_), t2_ = hi_ + dpp<CTRL>(hi_); xs[h] = (c & (BIT)) ? t2_ : t1_; }
      TR_STEP(8, XOR8, 8) TR_STEP(4, XOR7, 4) TR_STEP(2, XOR2, 2) TR_STEP(1, XOR1, 1)
#undef TR_STEP
      const float s = rbf(rbf(xs[0]) * 0.08838834764831845f);
      float m = s;
      m = fmaxf(m, dpp<XOR2>(m)); m = fmaxf(m, dpp<ROR4>(m)); m = fmaxf(m, dpp<XOR8>(m)); m = max16(m); m = max32(m);
      if (lane < 2) L[L_MW + lane * 8 + ww] = m;
      lds_arrive(LU + L_FLAG + F_MCNT, lane);
      lds_wait_ge<false>(FL + F_MCNT, 8);
      WSTAMP(3);
      const v4f ma = *(const LAS v4f*)(L + L_MW + (lane & 1) * 8), mb = *(const LAS v4f*)(L + L_MW + (lane & 1) * 8 + 4);
      m = fmaxf(fmaxf(fmaxf(ma.x, ma.y), fmaxf(ma.z, ma.w)), fmaxf(fmaxf(mb.x, mb.y), fmaxf(mb.z, mb.w)));
      const float p = __builtin_amdgcn_exp2f((s - m) * LOG2E);
      float l = p;
      l += dpp<XOR2>(l); l += dpp<ROR4>(l); l += dpp<XOR8>(l); l = tr16(l, l); l = tr32(l, l);
      if (lane < 2) L[L_LW + lane * 8 + ww] = l;
      const unsigned pb = asu(rbf(p));
      pp = pkhi(pb, pb);
    }
    v4u msk;
#pragma unroll
    for (int k = 0; k < 4; ++k) msk[k] = ((c & 7) >> 1) == k ? ((c & 1) ? 0xffff0000u : 0x0000ffffu) : 0u;
    v4f acc = {0.f, 0.f, 0.f, 0.f};
#define PV_STEP(j) { \
      int pj = __builtin_amdgcn_update_dpp(0, (int)pp, 0x150 + 2 * j, 0xf, 0x3, false); \
      pj = __builtin_amdgcn_update_dpp(pj, (int)pp, 0x150 + 2 * j + 1, 0xf, 0xc, false); \
      const v4u af = {msk[0] & pj, msk[1] & pj, msk[2] & pj, msk[3] & pj}; \
      acc = __builtin_amdgcn_mfma_f32_16x16x32_bf16(__builtin_bit_cast(bf16x8, af), __builtin_bit_cast(bf16x8, V[j]), acc, 0, 0, 0); }
    PV_STEP(0) PV_STEP(1) PV_STEP(2) PV_STEP(3) PV_STEP(4) PV_STEP(5) PV_STEP(6) PV_STEP(7)
#undef PV_STEP
    *(LAS v4f*)(L + L_OP + ww * 256 + (lane >> 5) * 128 + 8 * c + 4 * ((lane >> 4) & 1)) = acc;
    lds_arrive(LU + L_FLAG + F_OCNT, lane);
    WSTAMP(4);
    WDUMP();
    return;
  }

  if (wave == 1) {
    const auto rq = mk(wq, 576 * 4096 * 2);
    const int qa = gi >> 2, qr = gi & 3;
    const v4u xa = __builtin_amdgcn_raw_buffer_load_b128(rx, lane * 16, 0, 0);
    const unsigned xb = __builtin_amdgcn_raw_buffer_load_b16(rx, 1024 + lane * 2, 0, 0);
    const unsigned e = __builtin_amdgcn_raw_buffer_load_b32(rws, WS_E, 0, 16);
    v4u WQ[18];
    const unsigned offq = ((144 * qr + 2 * (lane >> 3)) * 4096 + 256 * g + 64 * qa + 8 * (lane & 7)) * 2;
#pragma unroll
    for (int i = 0; i < 18; ++i) WQ[i] = __builtin_amdgcn_raw_buffer_load_b128(rq, offq, ((i >> 1) * 16 + (i & 1)) * 8192, 2);
    __builtin_amdgcn_s_barrier();
    SB();
    float v[9];
#pragma unroll
    for (int k = 0; k < 4; ++k) { v[2 * k] = lo16(xa[k]); v[2 * k + 1] = hi16(xa[k]); }
    v[8] = lo16(xb);
    layernorm9(v);
    store9(LU + L_XN, v, lane);
    WSTAMP(1);
    float a[8];
#pragma unroll
    for (int k = 0; k < 8; ++k) a[k] = 0.f;
#pragma unroll
    for (int i = 0; i < 9; ++i) {
      const unsigned xp = LU[L_XN + 72 * qr + 8 * i + (lane >> 3)];
#pragma unroll
      for (int c = 0; c < 4; ++c) {
        a[2 * c] = dot2(pklo(WQ[2 * i][c], WQ[2 * i + 1][c]), xp, a[2 * c]);
        a[2 * c + 1] = dot2(pkhi(WQ[2 * i][c], WQ[2 * i + 1][c]), xp, a[2 * c + 1]);
      }
    }
    const float r = red8(a, lane);
    const unsigned tg = __builtin_amdgcn_readfirstlane(e) + 1u;
    const int col = 8 * (lane & 7) + ((lane >> 3) & 1) * 4 + ((lane >> 4) & 1) * 2 + (lane >> 5);
    STSC8((v2u{asu(r), tg}), rws, WS_Q + (u * 64 + col) * 8);
    asm volatile("" ::: "memory");
    if (lane == 0) { FL[F_TAG] = tg; FL[F_QPUB] = 1u; }
    WSTAMP(2);
  } else {
#pragma unroll
    for (int k = 0; k < 5; ++k) { const int i = (k * 4 + (wave - 11)) * 64 + lane; if (i < 18 * 64) *(LAS v4u*)(LU + L_FR + 4 * i) = v4u{0u, 0u, 0u, 0u}; }
    LGKM0();
    __builtin_amdgcn_s_barrier();
  }
  {
    const int r = lane >> 4, c = lane & 15;
    const auto ro = mk(wo + (size_t)u * 16 * D, 16 * D * 2), r2 = mk(w2 + (size_t)u * 18 * D, 18 * D * 2);
    v4u W1[9], WF[9];
    const bool has1 = wave >= 11 && wave < 14;
    if (has1) {
      const auto r1 = mk(w1, 576 * F * 2);
      const int hw = wave - 11;
      const unsigned off1 = c < 12 ? ((36 * gi + r) * F + 288 * g + 96 * hw + 8 * c) * 2 : OOB;
      lds_wait_ge<true>(FL + F_KVI, W1_AT + hw);
      WSTAMP(3);
#pragma unroll
      for (int k = 0; k < 9; ++k) W1[k] = __builtin_amdgcn_raw_buffer_load_b128(r1, off1, k * (4 * F * 2), 2);
      lds_arrive(LU + L_FLAG + F_W1I, lane);
    }
    const int cb = wave == 1 ? 512 : 128 * (wave - 11);
    const unsigned off2 = (wave != 1 || c < 8) ? (r * D + cb + 8 * c) * 2 : OOB;
    lds_wait_ge<true>(FL + F_KVI, WF_AT);
    lds_wait_ge<true>(FL + F_W1I, 3);
    WSTAMP(4);
#pragma unroll
    for (int k = 0; k < 4; ++k) WF[k] = __builtin_amdgcn_raw_buffer_load_b128(ro, off2, k * (4 * D * 2), 2);
#pragma unroll
    for (int k = 0; k < 5; ++k) WF[4 + k] = LDNT(r2, off2 + k * (4 * D * 2));
    SB();
    lds_wait_ge<true>(FL + F_Y, 1);
    const unsigned tag = FL[F_TAG];
    const int cl = 8 * c + 4 * (r & 1) + 2 * (r >> 1);
    if (has1) {
      v4f acc = {0.f, 0.f, 0.f, 0.f};
#pragma unroll
      for (int k = 0; k < 9; ++k)
        acc = __builtin_amdgcn_mfma_f32_16x16x32_bf16(__builtin_bit_cast(bf16x8, *(const LAS v4u*)(LU + L_FY + k * 256 + 4 * lane)), __builtin_bit_cast(bf16x8, W1[k]), acc, 0, 0, 0);
      const auto s0 = __builtin_amdgcn_permlane32_swap(asu(acc[0]), asu(acc[2]), false, false);
      const auto s1 = __builtin_amdgcn_permlane32_swap(asu(acc[1]), asu(acc[3]), false, false);
      STSC((v4u{s0[0], tag, s1[0], tag}), rws, c < 12 ? WS_H + (u * 288 + 96 * (wave - 11) + cl) * 8 : OOB);
      lds_arrive(LU + L_FLAG + F_HPUB, lane);
      WSTAMP(5);
    }
    const unsigned poff = (wave != 1 || c < 8) ? WS_P + (u * 576 + cb + cl) * 8 : OOB;
    v4f acc = {0.f, 0.f, 0.f, 0.f}, acc2 = {0.f, 0.f, 0.f, 0.f};
    lds_wait_ge<false>(FL + F_CO, 1);
#pragma unroll
    for (int k = 0; k < 4; ++k)
      acc = __builtin_amdgcn_mfma_f32_16x16x32_bf16(__builtin_bit_cast(bf16x8, *(const LAS v4u*)(LU + L_FR + k * 256 + 4 * lane)), __builtin_bit_cast(bf16x8, WF[k]), acc, 0, 0, 0);
    asm volatile("" : "+v"(acc));
    lds_wait_ge<false>(FL + F_CH, 1);
#pragma unroll
    for (int k = 4; k < 9; ++k) {
      v4f& a = (k & 1) ? acc2 : acc;
      a = __builtin_amdgcn_mfma_f32_16x16x32_bf16(__builtin_bit_cast(bf16x8, *(const LAS v4u*)(LU + L_FR + k * 256 + 4 * lane)), __builtin_bit_cast(bf16x8, WF[k]), a, 0, 0, 0);
    }
    acc += acc2;
    const auto s0 = __builtin_amdgcn_permlane32_swap(asu(acc[0]), asu(acc[2]), false, false);
    const auto s1 = __builtin_amdgcn_permlane32_swap(asu(acc[1]), asu(acc[3]), false, false);
    STSC((v4u{s0[0], tag, s1[0], tag}), rws, poff);
    if (lane == 0) FL[F_PPUB] = 1u;
    WSTAMP(7);
  }
  WDUMP();
}

extern "C" void kernel_launch(void* const* d_in, const int* in_sizes, int n_in,
                              void* d_out, int out_size, void* d_ws, size_t ws_size,
                              hipStream_t stream) {
  (void)hipFuncSetAttribute((const void*)fused_decode, hipFuncAttributeMaxDynamicSharedMemorySize, LDS_BYTES);
  fused_decode<<<256, NTHR, LDS_BYTES, stream>>>((const u16*)d_in[0], (const u16*)d_in[1], (const u16*)d_in[2], (const u16*)d_in[3],
                                                 (const u16*)d_in[4], (const u16*)d_in[6], (const u16*)d_in[7], (const u16*)d_in[8],
                                                 (const u16*)d_in[9], (u16*)d_out, (unsigned char*)d_ws);
}
```

```cpp
#include <hip/hip_runtime.h>
#include <hip/hip_bf16.h>
#include <cstdint>

typedef unsigned short u16;
typedef unsigned v4u __attribute__((ext_vector_type(4)));
typedef unsigned v2u __attribute__((ext_vector_type(2)));
typedef float v4f __attribute__((ext_vector_type(4)));
typedef float v2f __attribute__((ext_vector_type(2)));
typedef __bf16 bf16x2 __attribute__((ext_vector_type(2)));
typedef short bf16x8 __attribute__((ext_vector_type(8)));
#define LAS __attribute__((address_space(3)))

#define WIN 4
#define WWIN 7
#define NOPOLL 0
#define ILV 1
#define KSLEEP 20
#define W1_AT 8
#define WF_AT 8
#define PRIO 0
#define SLP 1
#define W1_EARLY 0
#define WF_STAG 0
#define TW1 560
#define TWF 620
#define TPO 820
#define TPH 820
#define TCATCH 520
#define WARMDIAG 0
#define STAG 1
#define KVI_AT 7
#define CODE_KB 12
#define DLY_Q 30
#define DLY_O 16
#define DLY_H 16
#define DLY_P 16

constexpr int D = 576, SK = 65536, W = 4096, F = 4608;
constexpr int NTHR = 960;
constexpr unsigned OOB = 0x80000000u;
constexpr unsigned WS_E = 0;
constexpr unsigned WS_Q = 4096;
constexpr unsigned WS_H = WS_Q + 256 * 64 * 8;
constexpr unsigned WS_O = WS_H + 256 * 288 * 8;
constexpr unsigned WS_M = WS_O + 256 * 256 * 8;
constexpr unsigned WS_P = WS_M + 256 * 128;
constexpr unsigned WS_END = WS_P + 256 * 576 * 8;
constexpr unsigned WS_DBG = 8u << 20;
constexpr int L_XN = 0, L_Y = L_XN + 288, L_QB = L_Y + 288, L_MW = L_QB + 128, L_LW = L_MW + 16, L_CO = L_LW + 16,
              L_OP = L_CO + 32, L_FLAG = L_OP + 8 * 256, L_FR = L_FLAG + 32, L_FY = L_FR + 9 * 256, L_FIN = L_FY + 9 * 256, L_KT = L_FIN + 32, L_END = L_KT + 16384;
constexpr int LDS_BYTES = 104 * 1024;
static_assert(L_END * 4 <= LDS_BYTES, "lds");
enum { F_T0 = 13, F_FCNT = 12, F_CH = 11, F_W1I = 10, F_KVI = 9, F_Y = 0, F_QPUB, F_Q, F_MCNT, F_OCNT, F_HPUB, F_CO, F_PPUB, F_TAG };
constexpr unsigned SPIN_CAP = 1u << 22;
constexpr float LOG2E = 1.4426950408889634f;

__device__ __forceinline__ float rbf(float f) {
  unsigned u = __float_as_uint(f);
  u = (u + 0x7fffu + ((u >> 16) & 1u)) & 0xffff0000u;
  return __uint_as_float(u);
}
__device__ __forceinline__ float lo16(unsigned w) { return __uint_as_float(w << 16); }
__device__ __forceinline__ float hi16(unsigned w) { return __uint_as_float(w & 0xffff0000u); }
__device__ __forceinline__ float asf(unsigned w) { return __uint_as_float(w); }
__device__ __forceinline__ unsigned asu(float w) { return __float_as_uint(w); }
__device__ __forceinline__ unsigned pklo(unsigned a, unsigned b) { return __builtin_amdgcn_perm(b, a, 0x05040100u); }
__device__ __forceinline__ unsigned pkhi(unsigned a, unsigned b) { return __builtin_amdgcn_perm(b, a, 0x07060302u); }
__device__ __forceinline__ float dot2(unsigned k, unsigned q, float acc) {
  return __builtin_amdgcn_fdot2_f32_bf16(__builtin_bit_cast(bf16x2, k), __builtin_bit_cast(bf16x2, q), acc, false);
}
template <int CTRL> __device__ __forceinline__ float dpp(float x) {
  return __builtin_bit_cast(float, __builtin_amdgcn_mov_dpp(__builtin_bit_cast(int, x), CTRL, 0xf, 0xf, true));
}
template <int CTRL> __device__ __forceinline__ unsigned dppu(unsigned x) { return (unsigned)__builtin_amdgcn_mov_dpp((int)x, CTRL, 0xf, 0xf, true); }
constexpr int XOR1 = 0xB1, XOR2 = 0x4E, XOR3 = 0x1B, XOR7 = 0x141, XOR8 = 0x128, ROR4 = 0x124;
__device__ __forceinline__ float tr16(float a, float b) {
  auto s = __builtin_amdgcn_permlane16_swap(asu(a), asu(b), false, false);
  return asf(s[0]) + asf(s[1]);
}
__device__ __forceinline__ float tr32(float a, float b) {
  auto s = __builtin_amdgcn_permlane32_swap(asu(a), asu(b), false, false);
  return asf(s[0]) + asf(s[1]);
}
__device__ __forceinline__ float max16(float a) {
  auto s = __builtin_amdgcn_permlane16_swap(asu(a), asu(a), false, false);
  return fmaxf(asf(s[0]), asf(s[1]));
}
__device__ __forceinline__ float max32(float a) {
  auto s = __builtin_amdgcn_permlane32_swap(asu(a), asu(a), false, false);
  return fmaxf(asf(s[0]), asf(s[1]));
}
__device__ __forceinline__ float wave_sum(float t) {
  t += dpp<XOR1>(t); t += dpp<XOR2>(t); t += dpp<ROR4>(t); t += dpp<XOR8>(t);
  t = tr16(t, t); t = tr32(t, t);
  return t;
}
__device__ __forceinline__ float red8(float (&a)[8], int lane) {
#pragma unroll
  for (int h = 0; h < 4; ++h) { const float t1 = a[h] + dpp<XOR8>(a[h]), t2 = a[h + 4] + dpp<XOR8>(a[h + 4]); a[h] = (lane & 8) ? t2 : t1; }
  a[0] = tr16(a[0], a[2]); a[1] = tr16(a[1], a[3]);
  return tr32(a[0], a[1]);
}
#define VMWAIT(n) asm volatile("s_waitcnt vmcnt(%0)" :: "n"(n) : "memory")
#define LGKM0() asm volatile("s_waitcnt lgkmcnt(0)" ::: "memory")
#define LDNT(r, off) __builtin_amdgcn_raw_buffer_load_b128((r), (off), 0, 2)
#define LDNT16(r, off) __builtin_amdgcn_raw_buffer_load_b16((r), (off), 0, 2)
#define LDSC(r, off, so) __builtin_amdgcn_raw_buffer_load_b128((r), (off), (so), 16)
#define LDSC8(r, off, so) __builtin_amdgcn_raw_buffer_load_b64((r), (off), (so), 16)
#define STSC(v, r, off) __builtin_amdgcn_raw_buffer_store_b128((v), (r), (off), 0, 16)
#define STSC8(v, r, off) __builtin_amdgcn_raw_buffer_store_b64((v), (r), (off), 0, 16)
#define SB() __builtin_amdgcn_sched_barrier(0)
#define PIN8(a) asm volatile("" : "+v"(a[0]), "+v"(a[1]), "+v"(a[2]), "+v"(a[3]), "+v"(a[4]), "+v"(a[5]), "+v"(a[6]), "+v"(a[7]))

__device__ __forceinline__ void wait_until(unsigned t0, unsigned dt) {
  while ((unsigned)__builtin_amdgcn_s_memrealtime() - t0 < dt) __builtin_amdgcn_s_sleep(1);
}
template <bool SLEEP> __device__ __forceinline__ void lds_wait_ge(volatile LAS unsigned* p, unsigned v) {
#pragma unroll 1
  for (unsigned n = 0; n < SPIN_CAP; ++n) { if (*p >= v) break; if (SLEEP) __builtin_amdgcn_s_sleep(SLP); }
  asm volatile("" ::: "memory");
}
__device__ __forceinline__ void lds_arrive(LAS unsigned* p, int lane) {
  asm volatile("" ::: "memory");
  if (lane == 0) (void)__hip_atomic_fetch_add(p, 1u, __ATOMIC_RELAXED, __HIP_MEMORY_SCOPE_WORKGROUP);
  asm volatile("" ::: "memory");
}

__device__ __forceinline__ void layernorm9(float (&v)[9]) {
  float s = ((v[0] + v[1]) + (v[2] + v[3])) + ((v[4] + v[5]) + (v[6] + v[7])) + v[8];
  const float mean = wave_sum(s) * (1.f / D);
  float s2 = 0.f, s3 = 0.f;
#pragma unroll
  for (int k = 0; k < 8; k += 2) { const float d = v[k] - mean, e = v[k + 1] - mean; s2 = fmaf(d, d, s2); s3 = fmaf(e, e, s3); }
  { const float d = v[8] - mean; s2 = fmaf(d, d, s2); }
  const float var = rbf(wave_sum(s2 + s3) * (1.f / D));
  const float mb = rbf(mean);
  const float den = rbf(__builtin_amdgcn_sqrtf(rbf(var + 1e-5f)));
  const float inv = __builtin_amdgcn_rcpf(den);
#pragma unroll
  for (int k = 0; k < 9; ++k) {
    const float n = rbf(v[k] - mb);
    float qv = n * inv;
    qv = fmaf(fmaf(-qv, den, n), inv, qv);
    v[k] = rbf(qv);
  }
}
__device__ __forceinline__ void store9(LAS unsigned* dst, const float (&v)[9], int lane) {
  *(LAS v4u*)(dst + 4 * lane) = v4u{pkhi(asu(v[0]), asu(v[1])), pkhi(asu(v[2]), asu(v[3])), pkhi(asu(v[4]), asu(v[5])), pkhi(asu(v[6]), asu(v[7]))};
  ((LAS u16*)dst)[512 + lane] = (u16)(asu(v[8]) >> 16);
}

#define WSTAMP(k) do {} while (0)
#define WDUMP() do {} while (0)

__global__ void __launch_bounds__(NTHR) fused_decode(const u16* __restrict__ x, const u16* __restrict__ kc, const u16* __restrict__ vc,
                                                     const u16* __restrict__ wq, const u16* __restrict__ wo, const u16* __restrict__ w1,
                                                     const u16* __restrict__ b1, const u16* __restrict__ w2, const u16* __restrict__ b2,
                                                     u16* __restrict__ out, unsigned char* ws) {
  const unsigned long long tk0 = __builtin_amdgcn_s_memrealtime();
  unsigned long long pc0;
  asm volatile("s_getpc_b64 %0" : "=s"(pc0));
  extern __shared__ __attribute__((aligned(16))) float smem[];
  LAS float* L = (LAS float*)smem;
  LAS unsigned* LU = (LAS unsigned*)smem;
  volatile LAS unsigned* FL = (volatile LAS unsigned*)(LU + L_FLAG);
  const int t = threadIdx.x, lane = t & 63;
  const int wave = __builtin_amdgcn_readfirstlane(t >> 6);
  const int u = blockIdx.x, g = u >> 4, gi = u & 15;
  auto mk = [](const void* p, unsigned bytes) { return __builtin_amdgcn_make_buffer_rsrc((void*)p, 0, bytes, 0x00020000); };
  const auto rws = mk(ws, WS_END);
  const auto rx = mk(x, D * 2);

  if (PRIO == 1 && (wave < 2 || wave >= 10)) __builtin_amdgcn_s_setprio(3);
  if (PRIO == 2 && wave < 2) __builtin_amdgcn_s_setprio(3);
  const int lg = (u >> 5) * 8 + (u & 7);
  const bool is_red = lg < 36;
  const int rcol = 16 * lg + 4 * ((u >> 3) & 3);
  if (wave == 0) {
    const int jj = u >> 3;
    const auto rc = mk((const void*)(pc0 & ~255ull), CODE_KB * 1024);
    const v4u cpf = __builtin_amdgcn_raw_buffer_load_b128(rc, (jj % CODE_KB) * 1024 + lane * 16, 0, 0);
    const auto rb2 = mk(b2, D * 2);
    const int oc = lane & 3;
    const unsigned xrr = __builtin_amdgcn_raw_buffer_load_b16(rx, is_red ? (rcol + oc) * 2 : OOB, 0, 0);
    const unsigned b2r = __builtin_amdgcn_raw_buffer_load_b16(rb2, is_red ? (rcol + oc) * 2 : OOB, 0, 0);
    if (lane < 32) LU[L_FLAG + lane] = lane == F_T0 ? (unsigned)tk0 : 0u;
    LGKM0();
    __builtin_amdgcn_s_barrier();
    SB();
    asm volatile("" :: "v"(cpf));
    lds_wait_ge<true>(FL + F_QPUB, 1);
    const unsigned tag = __builtin_amdgcn_readfirstlane(FL[F_TAG]);
    __builtin_amdgcn_s_sleep(DLY_Q);
    WSTAMP(1);
    {
      v4u qv[8];
      const unsigned qoff = WS_Q + g * 8192 + lane * 16;
#pragma unroll 1
      for (unsigned n = 0; n < SPIN_CAP; ++n) {
        asm volatile("" ::: "memory");
        unsigned bad = 0;
#pragma unroll
        for (int k = 0; k < 8; ++k) qv[k] = LDSC(rws, (NOPOLL & 1) ? OOB : qoff, k * 1024);
#pragma unroll
        for (int k = 0; k < 8; ++k) bad |= (qv[k].y ^ tag) | (qv[k].w ^ tag);
        if (__all(bad == 0) || (NOPOLL & 1)) break;
      }
      float s[8];
#pragma unroll
      for (int a = 0; a < 4; ++a) { s[2 * a] = asf(qv[2 * a].x) + asf(qv[2 * a + 1].x); s[2 * a + 1] = asf(qv[2 * a].z) + asf(qv[2 * a + 1].z); }
      const float r0 = tr32(s[0], s[4]), r1_ = tr32(s[1], s[5]), r2_ = tr32(s[2], s[6]), r3 = tr32(s[3], s[7]);
      const int a0 = (lane >> 5) * 2;
      LU[L_QB + a0 * 32 + (lane & 31)] = pkhi(asu(rbf(r0)), asu(rbf(r1_)));
      LU[L_QB + (a0 + 1) * 32 + (lane & 31)] = pkhi(asu(rbf(r2_)), asu(rbf(r3)));
      asm volatile("" ::: "memory");
      if (lane == 0) FL[F_Q] = 1u;
      WSTAMP(2);
    }
    lds_wait_ge<false>(FL + F_OCNT, 8);
    WSTAMP(3);
    {
      v2f oa = *(const LAS v2f*)(L + L_OP + 2 * lane), ob = *(const LAS v2f*)(L + L_OP + 128 + 2 * lane);
#pragma unroll
      for (int w = 1; w < 8; ++w) { oa += *(const LAS v2f*)(L + L_OP + w * 256 + 2 * lane); ob += *(const LAS v2f*)(L + L_OP + w * 256 + 128 + 2 * lane); }
      const int hd = lane & 1;
      const v4f la = *(const LAS v4f*)(L + L_LW + hd * 8), lb = *(const LAS v4f*)(L + L_LW + hd * 8 + 4);
      const v4f ma = *(const LAS v4f*)(L + L_MW + hd * 8), mb = *(const LAS v4f*)(L + L_MW + hd * 8 + 4);
      const float Ls = ((la.x + la.y) + (la.z + la.w)) + ((lb.x + lb.y) + (lb.z + lb.w));
      const float M = fmaxf(fmaxf(fmaxf(ma.x, ma.y), fmaxf(ma.z, ma.w)), fmaxf(fmaxf(mb.x, mb.y), fmaxf(mb.z, mb.w)));
      STSC((v4u{asu(oa.x), tag, asu(oa.y), tag}), rws, WS_O + u * 2048 + lane * 16);
      STSC((v4u{asu(ob.x), tag, asu(ob.y), tag}), rws, WS_O + u * 2048 + 1024 + lane * 16);
      STSC((v4u{asu(M), tag, asu(Ls), tag}), rws, lane < 8 ? WS_M + u * 128 + lane * 16 : OOB);
    }
    WSTAMP(4);
    __builtin_amdgcn_s_sleep(DLY_O);
    wait_until((unsigned)tk0, TPO);
    {
      const unsigned ooff = WS_O + (16 * g + (lane >> 4)) * 2048 + (16 * gi + (lane & 15)) * 8;
      const unsigned moff = WS_M + (16 * g + (lane >> 4)) * 128 + (gi >> 3) * 16;
      v2u ov[4]; v4u ml[4];
#pragma unroll 1
      for (unsigned n = 0; n < SPIN_CAP; ++n) {
        asm volatile("" ::: "memory");
        unsigned bad = 0;
#pragma unroll
        for (int k = 0; k < 4; ++k) { ov[k] = LDSC8(rws, (NOPOLL & 2) ? OOB : ooff, k * 8192); ml[k] = LDSC(rws, (NOPOLL & 2) ? OOB : moff, k * 512); }
#pragma unroll
        for (int k = 0; k < 4; ++k) bad |= (ov[k].y ^ tag) | (ml[k].y ^ tag) | (ml[k].w ^ tag);
        if (__all(bad == 0) || (NOPOLL & 2)) break;
      }
      WSTAMP(5);
      float M = fmaxf(fmaxf(asf(ml[0].x), asf(ml[1].x)), fmaxf(asf(ml[2].x), asf(ml[3].x)));
      M = max16(M); M = max32(M);
      float num = 0.f, den = 0.f;
#pragma unroll
      for (int k = 0; k < 4; ++k) { const float sc = __builtin_amdgcn_exp2f((asf(ml[k].x) - M) * LOG2E); num = fmaf(sc, asf(ov[k].x), num); den = fmaf(sc, asf(ml[k].z), den); }
      num = tr16(num, num); den = tr16(den, den); num = tr32(num, num); den = tr32(den, den);
      const float o = rbf(num * __builtin_amdgcn_rcpf(den));
      {
        const int d = lane & 15, m0 = 2 * (lane >> 4);
        LAS u16* fp = (LAS u16*)(LU + L_FR) + ((d >> 2) * 64 + (d & 3) * 16 + m0) * 8 + m0;
        fp[0] = (u16)(asu(o) >> 16); fp[9] = (u16)(asu(o) >> 16);
      }
      lds_arrive(LU + L_FLAG + F_CO, lane);
      WSTAMP(6);
    }
    if (is_red) {
      const float xb2 = lo16(xrr) + lo16(b2r);
      lds_wait_ge<false>(FL + F_FCNT, 8);
      float s0 = L[L_FIN + (lane & 31)];
      s0 += dpp<ROR4>(s0); s0 += dpp<XOR8>(s0); s0 = tr16(s0, s0);
      if (lane < 4) out[rcol + oc] = (u16)(asu(rbf(s0 + xb2)) >> 16);
      if (u == 0 && lane == 0) __builtin_amdgcn_raw_buffer_store_b32(tag, rws, WS_E, 0, 16);
      WSTAMP(7);
    }
    WDUMP();
    return;
  }
  if (wave == 10) {
    const auto rb1 = mk(b1, F * 2);
    const unsigned b1r = LDNT16(rb1, (lane & 31) < 18 ? (18 * u + (lane & 31)) * 2 : OOB);
    __builtin_amdgcn_s_barrier();
    lds_wait_ge<true>(FL + F_QPUB, 1);
    const unsigned tag = __builtin_amdgcn_readfirstlane(FL[F_TAG]);
    {
      const v4u xa = *(const LAS v4u*)(LU + L_XN + 4 * lane);
      const unsigned xb = ((const LAS u16*)(LU + L_XN))[512 + lane];
      float v[9];
#pragma unroll
      for (int k = 0; k < 4; ++k) { v[2 * k] = lo16(xa[k]); v[2 * k + 1] = hi16(xa[k]); }
      v[8] = lo16(xb);
      layernorm9(v);
      store9(LU + L_Y, v, lane);
#pragma unroll
      for (int it = 0; it < 5; ++it) {
        const int idx = it * 64 + lane, n = idx < 288 ? idx >> 3 : 35, m = idx & 7;
        ((LAS u16*)(LU + L_FY))[((n >> 2) * 64 + (n & 3) * 16 + m) * 8 + m] = ((const LAS u16*)(LU + L_Y))[36 * gi + n];
      }
    }
    asm volatile("" ::: "memory");
    if (lane == 0) FL[F_Y] = 1u;
    WSTAMP(1);
    const unsigned hoff = (lane & 31) < 18 ? WS_H + ((16 * g + (lane >> 5)) * 288 + 18 * gi + (lane & 31)) * 8 : OOB;
    const unsigned tagx = (lane & 31) < 18 ? tag : 0u;
    lds_wait_ge<true>(FL + F_HPUB, 3);
    __builtin_amdgcn_s_sleep(DLY_H);
    wait_until(__builtin_amdgcn_readfirstlane(FL[F_T0]), TPH);
    WSTAMP(2);
    v2u hv[8];
#pragma unroll 1
    for (unsigned n = 0; n < SPIN_CAP; ++n) {
      asm volatile("" ::: "memory");
      unsigned bad = 0;
#pragma unroll
      for (int k = 0; k < 8; ++k) hv[k] = LDSC8(rws, (NOPOLL & 2) ? OOB : hoff, k * (2 * 288 * 8));
#pragma unroll
      for (int k = 0; k < 8; ++k) bad |= hv[k].y ^ tagx;
      if (__all(bad == 0) || (NOPOLL & 2)) break;
    }
    WSTAMP(3);
    float pre = ((asf(hv[0].x) + asf(hv[1].x)) + (asf(hv[2].x) + asf(hv[3].x))) + ((asf(hv[4].x) + asf(hv[5].x)) + (asf(hv[6].x) + asf(hv[7].x)));
    pre = tr32(pre, pre);
    const float tt = rbf(rbf(pre) + lo16(b1r));
    const float inner = 0.7978845608028654f * (tt + 0.044715f * tt * tt * tt);
    const float th = 1.f - 2.f * __builtin_amdgcn_rcpf(__builtin_amdgcn_exp2f(inner * (2.f * LOG2E)) + 1.f);
    const float h = rbf(0.5f * tt * (1.f + th));
    if ((lane & 31) < 18) {
      const int n = 16 + (lane & 31), m0 = 4 * (lane >> 5);
      LAS u16* fp = (LAS u16*)(LU + L_FR) + ((n >> 2) * 64 + (n & 3) * 16 + m0) * 8 + m0;
      fp[0] = (u16)(asu(h) >> 16); fp[9] = (u16)(asu(h) >> 16); fp[18] = (u16)(asu(h) >> 16); fp[27] = (u16)(asu(h) >> 16);
    }
    lds_arrive(LU + L_FLAG + F_CO, lane);
    WSTAMP(4);
    WDUMP();
    return;
  }

  if (wave >= 2 && wave < 10) {
    const int ww = wave - 2;
    const size_t kvb = ((size_t)g * SK + (SK - W) + gi * 256) * 128;
    const auto rk = mk(kc + kvb, 65536), rv = mk(vc + kvb, 65536);
    v4u K[8], V[8];
    const unsigned kvoff = ILV ? ww * 1024 + lane * 16 : (ww * 32 * 128 + lane * 8) * 2;
    constexpr int JS = ILV ? 8192 : 1024;
    __builtin_amdgcn_s_sleep(KSLEEP);
#pragma unroll
    for (int j = 0; j < 8; ++j) { if (j == WIN) __builtin_amdgcn_s_barrier(); if (j >= WIN) VMWAIT(WIN - 1); K[j] = __builtin_amdgcn_raw_buffer_load_b128(rk, kvoff, j * JS, 2); }
    const int c = lane & 15, r = lane >> 4;
    LAS unsigned* kt = LU + L_KT + ww * 2048;
#define GATED() { if ((unsigned)__builtin_amdgcn_s_memrealtime() - (unsigned)tk0 < TCATCH) VMWAIT(WIN - 1); }
#pragma unroll
    for (int j = 0; j < 8; ++j) {
      GATED() V[j] = __builtin_amdgcn_raw_buffer_load_b128(rv, kvoff, j * JS, 2);
      SB();
    }
    lds_arrive(LU + L_FLAG + F_KVI, lane);
#pragma unroll
    for (int j = 0; j < 8; ++j) *(LAS v4u*)(kt + (4 * j + r) * 64 + ((c ^ (2 * j + (r & 1))) * 4)) = K[j];
#pragma unroll
    for (int f = 0; f < 8; ++f) K[f] = *(const LAS v4u*)(kt + (4 * (c >> 1) + 2 * (f >> 2) + (c & 1)) * 64 + (((4 * (f & 3) + r) ^ c) * 4));
    PIN8(K);
    WSTAMP(1);
    lds_wait_ge<true>(FL + F_Q, 1);
    WSTAMP(2);
    unsigned pp;
    {
      v4f t0 = {0.f, 0.f, 0.f, 0.f}, t1 = {0.f, 0.f, 0.f, 0.f};
#pragma unroll
      for (int k = 0; k < 4; ++k) {
        const bf16x8 qf = __builtin_bit_cast(bf16x8, *(const LAS v4u*)(LU + L_QB + (c & 1) * 64 + 16 * k + 4 * r));
        t0 = __builtin_amdgcn_mfma_f32_16x16x32_bf16(qf, __builtin_bit_cast(bf16x8, K[k]), t0, 0, 0, 0);
        t1 = __builtin_amdgcn_mfma_f32_16x16x32_bf16(qf, __builtin_bit_cast(bf16x8, K[4 + k]), t1, 0, 0, 0);
      }
      const float a0 = (r & 2) ? t1[0] : t0[0], a1 = (r & 2) ? t1[1] : t0[1];
      const float keep = (c & 1) ? a1 : a0, send = (c & 1) ? a0 : a1;
      const float got = dpp<XOR1>(send);
      const float sd = ((r ^ c) & 1) ? got : keep;
      const float s = rbf(rbf(sd) * 0.08838834764831845f);
      float m = s;
      m = fmaxf(m, dpp<XOR2>(m)); m = fmaxf(m, dpp<ROR4>(m)); m = fmaxf(m, dpp<XOR8>(m)); m = max16(m); m = max32(m);
      if (lane < 2) L[L_MW + lane * 8 + ww] = m;
      lds_arrive(LU + L_FLAG + F_MCNT, lane);
      lds_wait_ge<true>(FL + F_MCNT, 8);
      WSTAMP(3);
      const v4f ma = *(const LAS v4f*)(L + L_MW + (lane & 1) * 8), mb = *(const LAS v4f*)(L + L_MW + (lane & 1) * 8 + 4);
      m = fmaxf(fmaxf(fmaxf(ma.x, ma.y), fmaxf(ma.z, ma.w)), fmaxf(fmaxf(mb.x, mb.y), fmaxf(mb.z, mb.w)));
      const float p = __builtin_amdgcn_exp2f((s - m) * LOG2E);
      float l = p;
      l += dpp<XOR2>(l); l += dpp<ROR4>(l); l += dpp<XOR8>(l); l = tr16(l, l); l = tr32(l, l);
      if (lane < 2) L[L_LW + lane * 8 + ww] = l;
      const unsigned pb = asu(rbf(p));
      pp = pkhi(pb, pb);
    }
    v4u msk;
#pragma unroll
    for (int k = 0; k < 4; ++k) msk[k] = ((c & 7) >> 1) == k ? ((c & 1) ? 0xffff0000u : 0x0000ffffu) : 0u;
    v4f acc = {0.f, 0.f, 0.f, 0.f};
#define PV_STEP(j) { \
      int pj = __builtin_amdgcn_update_dpp(0, (int)pp, 0x150 + 2 * j, 0xf, 0x3, false); \
      pj = __builtin_amdgcn_update_dpp(pj, (int)pp, 0x150 + 2 * j + 1, 0xf, 0xc, false); \
      const v4u af = {msk[0] & pj, msk[1] & pj, msk[2] & pj, msk[3] & pj}; \
      acc = __builtin_amdgcn_mfma_f32_16x16x32_bf16(__builtin_bit_cast(bf16x8, af), __builtin_bit_cast(bf16x8, V[j]), acc, 0, 0, 0); }
    PV_STEP(0) PV_STEP(1) PV_STEP(2) PV_STEP(3) PV_STEP(4) PV_STEP(5) PV_STEP(6) PV_STEP(7)
#undef PV_STEP
    *(LAS v4f*)(L + L_OP + ww * 256 + (lane >> 5) * 128 + 8 * c + 4 * ((lane >> 4) & 1)) = acc;
    lds_arrive(LU + L_FLAG + F_OCNT, lane);
    WSTAMP(4);
    if (is_red) {
      const unsigned off = WS_P + ((32 * ww + (lane >> 1)) * 576 + rcol + 2 * (lane & 1)) * 8;
      const unsigned tag = FL[F_TAG];
      lds_wait_ge<true>(FL + F_PPUB, 1);
      __builtin_amdgcn_s_sleep(DLY_P);
      WSTAMP(5);
      v4u pv;
#pragma unroll 1
      for (unsigned n = 0; n < SPIN_CAP; ++n) {
        asm volatile("" ::: "memory");
        pv = LDSC(rws, (NOPOLL & 4) ? OOB : off, 0);
        if (__all(((pv.y ^ tag) | (pv.w ^ tag)) == 0) || (NOPOLL & 4)) break;
      }
      WSTAMP(6);
      float s0 = tr32(asf(pv.x), asf(pv.z));
      s0 += dpp<XOR2>(s0); s0 += dpp<ROR4>(s0); s0 += dpp<XOR8>(s0); s0 = tr16(s0, s0);
      if ((lane & 30) == 0) L[L_FIN + ww * 4 + 2 * (lane & 1) + (lane >> 5)] = s0;
      lds_arrive(LU + L_FLAG + F_FCNT, lane);
    }
    WDUMP();
    return;
  }

  if (wave == 1) {
    const auto rq = mk(wq, 576 * 4096 * 2);
    const int qa = gi >> 2, qr = gi & 3;
    const v4u xa = __builtin_amdgcn_raw_buffer_load_b128(rx, lane * 16, 0, 0);
    const unsigned xb = __builtin_amdgcn_raw_buffer_load_b16(rx, 1024 + lane * 2, 0, 0);
    const unsigned e = __builtin_amdgcn_raw_buffer_load_b32(rws, WS_E, 0, 16);
    v4u WQ[18];
    const unsigned offq = ((144 * qr + 2 * (lane >> 3)) * 4096 + 256 * g + 64 * qa + 8 * (lane & 7)) * 2;
#pragma unroll
    for (int i = 0; i < 18; ++i) WQ[i] = __builtin_amdgcn_raw_buffer_load_b128(rq, offq, ((i >> 1) * 16 + (i & 1)) * 8192, 2);
    __builtin_amdgcn_s_barrier();
    SB();
    float v[9];
#pragma unroll
    for (int k = 0; k < 4; ++k) { v[2 * k] = lo16(xa[k]); v[2 * k + 1] = hi16(xa[k]); }
    v[8] = lo16(xb);
    layernorm9(v);
    store9(LU + L_XN, v, lane);
    WSTAMP(1);
    float a[8];
#pragma unroll
    for (int k = 0; k < 8; ++k) a[k] = 0.f;
#pragma unroll
    for (int i = 0; i < 9; ++i) {
      const unsigned xp = LU[L_XN + 72 * qr + 8 * i + (lane >> 3)];
#pragma unroll
      for (int c = 0; c < 4; ++c) {
        a[2 * c] = dot2(pklo(WQ[2 * i][c], WQ[2 * i + 1][c]), xp, a[2 * c]);
        a[2 * c + 1] = dot2(pkhi(WQ[2 * i][c], WQ[2 * i + 1][c]), xp, a[2 * c + 1]);
      }
    }
    const float r = red8(a, lane);
    const unsigned tg = __builtin_amdgcn_readfirstlane(e) + 1u;
    const int col = 8 * (lane & 7) + ((lane >> 3) & 1) * 4 + ((lane >> 4) & 1) * 2 + (lane >> 5);
    STSC8((v2u{asu(r), tg}), rws, WS_Q + (u * 64 + col) * 8);
    asm volatile("" ::: "memory");
    if (lane == 0) { FL[F_TAG] = tg; FL[F_QPUB] = 1u; }
    WSTAMP(2);
  } else {
#pragma unroll
    for (int k = 0; k < 5; ++k) { const int i = (k * 4 + (wave - 11)) * 64 + lane; if (i < 18 * 64) *(LAS v4u*)(LU + L_FR + 4 * i) = v4u{0u, 0u, 0u, 0u}; }
    LGKM0();
    __builtin_amdgcn_s_barrier();
  }
  {
    const int r = lane >> 4, c = lane & 15;
    const auto ro = mk(wo + (size_t)u * 16 * D, 16 * D * 2), r2 = mk(w2 + (size_t)u * 18 * D, 18 * D * 2);
    v4u W1[9], WF[9];
    const bool has1 = wave >= 11 && wave < 14;
    const unsigned tb0 = __builtin_amdgcn_readfirstlane(FL[F_T0]);
    if (has1) {
      const auto r1 = mk(w1, 576 * F * 2);
      const int hw = wave - 11;
      const unsigned off1 = c < 12 ? ((36 * gi + r) * F + 288 * g + 96 * hw + 8 * c) * 2 : OOB;
      if (!W1_EARLY) { wait_until(tb0, TW1); lds_wait_ge<true>(FL + F_KVI, W1_AT); }
      WSTAMP(3);
#pragma unroll
      for (int k = 0; k < 9; ++k) W1[k] = __builtin_amdgcn_raw_buffer_load_b128(r1, off1, k * (4 * F * 2), 2);
    }
    const int cb = wave == 1 ? 512 : 128 * (wave - 11);
    const unsigned off2 = (wave != 1 || c < 8) ? (r * D + cb + 8 * c) * 2 : OOB;
    const int cl = 8 * c + 4 * (r & 1) + 2 * (r >> 1);
    auto hpart = [&]() {
      lds_wait_ge<true>(FL + F_Y, 1);
      const unsigned tag = FL[F_TAG];
      v4f acc = {0.f, 0.f, 0.f, 0.f};
#pragma unroll
      for (int k = 0; k < 9; ++k)
        acc = __builtin_amdgcn_mfma_f32_16x16x32_bf16(__builtin_bit_cast(bf16x8, *(const LAS v4u*)(LU + L_FY + k * 256 + 4 * lane)), __builtin_bit_cast(bf16x8, W1[k]), acc, 0, 0, 0);
      const auto s0 = __builtin_amdgcn_permlane32_swap(asu(acc[0]), asu(acc[2]), false, false);
      const auto s1 = __builtin_amdgcn_permlane32_swap(asu(acc[1]), asu(acc[3]), false, false);
      STSC((v4u{s0[0], tag, s1[0], tag}), rws, c < 12 ? WS_H + (u * 288 + 96 * (wave - 11) + cl) * 8 : OOB);
      lds_arrive(LU + L_FLAG + F_HPUB, lane);
      WSTAMP(5);
    };
    if (W1_EARLY && has1) hpart();
    wait_until(tb0, TWF);
    lds_wait_ge<true>(FL + F_KVI, WF_STAG ? (wave == 1 ? 1 : 2 * (wave - 10)) : WF_AT);
    WSTAMP(4);
#pragma unroll
    for (int k = 0; k < 5; ++k) WF[4 + k] = LDNT(r2, off2 + k * (4 * D * 2));
#pragma unroll
    for (int k = 0; k < 4; ++k) WF[k] = __builtin_amdgcn_raw_buffer_load_b128(ro, off2, k * (4 * D * 2), 2);
    SB();
    if (!W1_EARLY && has1) hpart();
    lds_wait_ge<true>(FL + F_Y, 1);
    const unsigned tag = FL[F_TAG];
    const unsigned poff = (wave != 1 || c < 8) ? WS_P + (u * 576 + cb + cl) * 8 : OOB;
    v4f acc = {0.f, 0.f, 0.f, 0.f}, acc2 = {0.f, 0.f, 0.f, 0.f};
    v4f acc3 = {0.f, 0.f, 0.f, 0.f};
    lds_wait_ge<false>(FL + F_CO, 2);
#pragma unroll
    for (int k = 0; k < 9; ++k) {
      v4f& a = (k % 3 == 0) ? acc : (k % 3 == 1) ? acc2 : acc3;
      a = __builtin_amdgcn_mfma_f32_16x16x32_bf16(__builtin_bit_cast(bf16x8, *(const LAS v4u*)(LU + L_FR + k * 256 + 4 * lane)), __builtin_bit_cast(bf16x8, WF[k]), a, 0, 0, 0);
    }
    acc += acc3;
    acc += acc2;
    const auto s0 = __builtin_amdgcn_permlane32_swap(asu(acc[0]), asu(acc[2]), false, false);
    const auto s1 = __builtin_amdgcn_permlane32_swap(asu(acc[1]), asu(acc[3]), false, false);
    STSC((v4u{s0[0], tag, s1[0], tag}), rws, poff);
    if (lane == 0) FL[F_PPUB] = 1u;
    WSTAMP(7);
  }
  WDUMP();
}

extern "C" void kernel_launch(void* const* d_in, const int* in_sizes, int n_in,
                              void* d_out, int out_size, void* d_ws, size_t ws_size,
                              hipStream_t stream) {
  (void)hipFuncSetAttribute((const void*)fused_decode, hipFuncAttributeMaxDynamicSharedMemorySize, LDS_BYTES);
  fused_decode<<<256, NTHR, LDS_BYTES, stream>>>((const u16*)d_in[0], (const u16*)d_in[1], (const u16*)d_in[2], (const u16*)d_in[3],
                                                 (const u16*)d_in[4], (const u16*)d_in[6], (const u16*)d_in[7], (const u16*)d_in[8],
                                                 (const u16*)d_in[9], (u16*)d_out, (unsigned char*)d_ws);
}
```

```cpp
#include <hip/hip_runtime.h>
#include <hip/hip_bf16.h>
#include <cstdint>

typedef unsigned short u16;
typedef unsigned v4u __attribute__((ext_vector_type(4)));
typedef unsigned v2u __attribute__((ext_vector_type(2)));
typedef float v4f __attribute__((ext_vector_type(4)));
typedef float v2f __attribute__((ext_vector_type(2)));
typedef __bf16 bf16x2 __attribute__((ext_vector_type(2)));
typedef short bf16x8 __attribute__((ext_vector_type(8)));
#define LAS __attribute__((address_space(3)))

#define CODE_KB 14
#define KSLEEP 10
#define DLY_Q 40
#define DLY_O 16
#define DLY_H 16
#define DLY_P 20
#define TW1 560
#define TWF 620
#define TWF2 800
#define TPO 740
#define TPH 740
#define TCATCH 520
#define TDRY 200
#define DRYQ 0
#define DRYRUN 0

constexpr int D = 576, SK = 65536, W = 4096, F = 4608;
constexpr int NTHR = 960;
constexpr unsigned OOB = 0x80000000u;
constexpr unsigned WS_E = 0;
constexpr unsigned WS_Q = 4096;
constexpr unsigned WS_H = WS_Q + 256 * 64 * 8;
constexpr unsigned WS_O = WS_H + 256 * 288 * 8;
constexpr unsigned WS_M = WS_O + 256 * 256 * 8;
constexpr unsigned WS_P = WS_M + 256 * 128;
constexpr unsigned WS_END = WS_P + 256 * 576 * 8;
constexpr unsigned WS_DBG = 8u << 20;
constexpr int L_XN = 0, L_Y = L_XN + 288, L_QB = L_Y + 288, L_MW = L_QB + 128, L_LW = L_MW + 16, L_OP = L_LW + 16, L_FLAG = L_OP + 4 * 256,
              L_FR = L_FLAG + 32, L_FY = L_FR + 9 * 256, L_FIN = L_FY + 9 * 256, L_PP = L_FIN + 32, L_KT = L_PP + 512, L_END = L_KT + 16384,
              L_DRY = L_END, L_TOT = L_DRY + L_KT + 4096;
constexpr int LDS_BYTES = L_TOT * 4;
static_assert(LDS_BYTES <= 160 * 1024 && LDS_BYTES > 80 * 1024, "one workgroup per CU");
enum { F_Y = 0, F_QPUB, F_Q, F_MCNT, F_OCNT, F_HPUB, F_CO, F_PPUB, F_TAG, F_KVI, F_FCNT, F_T0, F_POLL, F_P0 = 16, F_K0 = 20 };
enum { B_A = 1, B_Q, B_K, B_V, B_B, B_W, B_R, B_DRY = 8 };
constexpr unsigned SPIN_CAP = 1u << 22;
constexpr float LOG2E = 1.4426950408889634f;

__device__ __forceinline__ float rbf(float f) {
  unsigned u = __float_as_uint(f);
  u = (u + 0x7fffu + ((u >> 16) & 1u)) & 0xffff0000u;
  return __uint_as_float(u);
}
__device__ __forceinline__ float lo16(unsigned w) { return __uint_as_float(w << 16); }
__device__ __forceinline__ float hi16(unsigned w) { return __uint_as_float(w & 0xffff0000u); }
__device__ __forceinline__ float asf(unsigned w) { return __uint_as_float(w); }
__device__ __forceinline__ unsigned asu(float w) { return __float_as_uint(w); }
__device__ __forceinline__ unsigned pklo(unsigned a, unsigned b) { return __builtin_amdgcn_perm(b, a, 0x05040100u); }
__device__ __forceinline__ unsigned pkhi(unsigned a, unsigned b) { return __builtin_amdgcn_perm(b, a, 0x07060302u); }
__device__ __forceinline__ float dot2(unsigned k, unsigned q, float acc) {
  return __builtin_amdgcn_fdot2_f32_bf16(__builtin_bit_cast(bf16x2, k), __builtin_bit_cast(bf16x2, q), acc, false);
}
__device__ __forceinline__ v4f mfma(v4u a, v4u b, v4f c) {
  return __builtin_amdgcn_mfma_f32_16x16x32_bf16(__builtin_bit_cast(bf16x8, a), __builtin_bit_cast(bf16x8, b), c, 0, 0, 0);
}
template <int CTRL> __device__ __forceinline__ float dpp(float x) {
  return __builtin_bit_cast(float, __builtin_amdgcn_mov_dpp(__builtin_bit_cast(int, x), CTRL, 0xf, 0xf, true));
}
constexpr int XOR1 = 0xB1, XOR2 = 0x4E, XOR8 = 0x128, ROR4 = 0x124;
__device__ __forceinline__ float tr16(float a, float b) {
  auto s = __builtin_amdgcn_permlane16_swap(asu(a), asu(b), false, false);
  return asf(s[0]) + asf(s[1]);
}
__device__ __forceinline__ float tr32(float a, float b) {
  auto s = __builtin_amdgcn_permlane32_swap(asu(a), asu(b), false, false);
  return asf(s[0]) + asf(s[1]);
}
__device__ __forceinline__ float max16(float a) {
  auto s = __builtin_amdgcn_permlane16_swap(asu(a), asu(a), false, false);
  return fmaxf(asf(s[0]), asf(s[1]));
}
__device__ __forceinline__ float max32(float a) {
  auto s = __builtin_amdgcn_permlane32_swap(asu(a), asu(a), false, false);
  return fmaxf(asf(s[0]), asf(s[1]));
}
__device__ __forceinline__ float wave_sum(float t) {
  t += dpp<XOR1>(t); t += dpp<XOR2>(t); t += dpp<ROR4>(t); t += dpp<XOR8>(t);
  t = tr16(t, t); t = tr32(t, t);
  return t;
}
__device__ __forceinline__ float red8(float (&a)[8], int lane) {
#pragma unroll
  for (int h = 0; h < 4; ++h) { const float t1 = a[h] + dpp<XOR8>(a[h]), t2 = a[h + 4] + dpp<XOR8>(a[h + 4]); a[h] = (lane & 8) ? t2 : t1; }
  a[0] = tr16(a[0], a[2]); a[1] = tr16(a[1], a[3]);
  return tr32(a[0], a[1]);
}
#define VMWAIT(n) asm volatile("s_waitcnt vmcnt(%0)" :: "n"(n) : "memory")
#define LGKM0() asm volatile("s_waitcnt lgkmcnt(0)" ::: "memory")
#define MEMBAR() asm volatile("" ::: "memory")
#define LDNT(r, off, so) __builtin_amdgcn_raw_buffer_load_b128((r), (off), (so), 2)
#define LDSC(r, off, so) __builtin_amdgcn_raw_buffer_load_b128((r), (off), (so), 16)
#define LDSC8(r, off, so) __builtin_amdgcn_raw_buffer_load_b64((r), (off), (so), 16)
#define STSC(v, r, off) __builtin_amdgcn_raw_buffer_store_b128((v), (r), (off), 0, 16)
#define STSC8(v, r, off) __builtin_amdgcn_raw_buffer_store_b64((v), (r), (off), 0, 16)
#define SB() __builtin_amdgcn_sched_barrier(0)
#define PIN8(a) asm volatile("" : "+v"((a)[0]), "+v"((a)[1]), "+v"((a)[2]), "+v"((a)[3]), "+v"((a)[4]), "+v"((a)[5]), "+v"((a)[6]), "+v"((a)[7]))

__device__ __forceinline__ void layernorm9(float (&v)[9]) {
  float s = ((v[0] + v[1]) + (v[2] + v[3])) + ((v[4] + v[5]) + (v[6] + v[7])) + v[8];
  const float mean = wave_sum(s) * (1.f / D);
  float s2 = 0.f, s3 = 0.f;
#pragma unroll
  for (int k = 0; k < 8; k += 2) { const float d = v[k] - mean, e = v[k + 1] - mean; s2 = fmaf(d, d, s2); s3 = fmaf(e, e, s3); }
  { const float d = v[8] - mean; s2 = fmaf(d, d, s2); }
  const float var = rbf(wave_sum(s2 + s3) * (1.f / D));
  const float mb = rbf(mean);
  const float den = rbf(__builtin_amdgcn_sqrtf(rbf(var + 1e-5f)));
  const float inv = __builtin_amdgcn_rcpf(den);
#pragma unroll
  for (int k = 0; k < 9; ++k) {
    const float n = rbf(v[k] - mb);
    float qv = n * inv;
    qv = fmaf(fmaf(-qv, den, n), inv, qv);
    v[k] = rbf(qv);
  }
}
__device__ __forceinline__ void store9(LAS unsigned* dst, const float (&v)[9], int lane) {
  *(LAS v4u*)(dst + 4 * lane) = v4u{pkhi(asu(v[0]), asu(v[1])), pkhi(asu(v[2]), asu(v[3])), pkhi(asu(v[4]), asu(v[5])), pkhi(asu(v[6]), asu(v[7]))};
  ((LAS u16*)dst)[512 + lane] = (u16)(asu(v[8]) >> 16);
}

#define WSTAMP(k) do {} while (0)

__global__ void __launch_bounds__(NTHR) fused_decode(const u16* __restrict__ x, const u16* __restrict__ kc, const u16* __restrict__ vc,
                                                     const u16* __restrict__ wq, const u16* __restrict__ wo, const u16* __restrict__ w1,
                                                     const u16* __restrict__ b1, const u16* __restrict__ w2, const u16* __restrict__ b2,
                                                     u16* __restrict__ out, unsigned char* ws) {
  const unsigned tk0 = (unsigned)__builtin_amdgcn_s_memrealtime();
  unsigned long long pc0;
  asm volatile("s_getpc_b64 %0" : "=s"(pc0));
  extern __shared__ __attribute__((aligned(16))) float smem[];
  const int t = threadIdx.x;
  int lane = t & 63;
  int wave = __builtin_amdgcn_readfirstlane(t >> 6);
  int u = blockIdx.x;
  int g = u >> 4, gi = u & 15;
  auto mk = [](const void* p, unsigned bytes) { return __builtin_amdgcn_make_buffer_rsrc((void*)p, 0, bytes, 0x00020000); };
  const auto rws = mk(ws, WS_END);
  v4u R[18];
  v4u xa; unsigned xb, ew;
#pragma unroll
  for (int i = 0; i < 18; ++i) asm volatile("" : "=v"(R[i]));
  asm volatile("" : "=v"(xa), "=v"(xb), "=v"(ew));
  if (wave == 1) {
    const auto rx = mk(x, D * 2), rq = mk(wq, 576 * 4096 * 2);
    xa = __builtin_amdgcn_raw_buffer_load_b128(rx, lane * 16, 0, 0);
    xb = __builtin_amdgcn_raw_buffer_load_b16(rx, 1024 + lane * 2, 0, 0);
    ew = __builtin_amdgcn_raw_buffer_load_b32(rws, WS_E, 0, 16);
    const unsigned offq = ((144 * (gi & 3) + 2 * (lane >> 3)) * 4096 + 256 * g + 64 * (gi >> 2) + 8 * (lane & 7)) * 2;
#pragma unroll
    for (int i = 0; i < 18; ++i) R[i] = LDNT(rq, offq, ((i >> 1) * 16 + (i & 1)) * 8192);
  }
  int wv0 = wave;
  asm volatile("" : "+s"(wv0));
  if (wv0 == 0) {
    const auto rc = mk((const void*)(pc0 & ~255ull), CODE_KB * 1024);
    R[0] = __builtin_amdgcn_raw_buffer_load_b128(rc, ((u >> 3) % CODE_KB) * 1024 + lane * 16, 0, 0);
    if (lane < 32) ((LAS unsigned*)smem)[L_FLAG + lane] = lane == F_T0 ? tk0 : 0u;
  }
  asm volatile("" : "+s"(wv0));
  if (wv0 >= 10) {
#pragma unroll
    for (int k = 0; k < 4; ++k) *(LAS v4u*)((LAS unsigned*)smem + L_FR + 4 * ((k * 5 + (wv0 - 10)) * 64 + lane)) = v4u{0u, 0u, 0u, 0u};
  }
  unsigned kvo = ((wave - 2) & 3) * 1024 + lane * 16;
  asm volatile("" : "+v"(kvo));
  LGKM0();
  SB(); __builtin_amdgcn_s_barrier(); SB();
  if (wave >= 2 && wave < 6) {
    const auto rk = mk(kc + ((size_t)g * SK + (SK - W) + gi * 256) * 128, 65536);
    __builtin_amdgcn_s_sleep(KSLEEP);
#pragma unroll
    for (int j = 0; j < 8; ++j) R[j] = LDNT(rk, kvo, j * 4096);
  }
  SB();
  if (wave == 1 || (wave >= 2 && wave < 6) || (DRYRUN && ((DRYQ && wave == 6) || wave == 7))) {
    const bool dry = wave >= 6;
    if (wave == 7) while ((unsigned)__builtin_amdgcn_s_memrealtime() - tk0 < TDRY) __builtin_amdgcn_s_sleep(2);
    g = u >> 4; gi = u & 15;
    const int c = lane & 15, r = lane >> 4;
    const size_t kvb = ((size_t)g * SK + (SK - W) + gi * 256) * 128;
    const int kw = (wave - 2) & 3;
    const unsigned kvoff = kw * 1024 + lane * 16;
    const int lg = (u >> 5) * 8 + (u & 7);
    const bool is_red = lg < 36;
    const int rcol = 16 * lg + 4 * ((u >> 3) & 3);
    LAS float* L = (LAS float*)smem + (dry ? L_DRY : 0);
    LAS unsigned* LU = (LAS unsigned*)L;
    volatile LAS unsigned* FL = (volatile LAS unsigned*)(LU + L_FLAG);
    const unsigned gm = dry ? OOB : 0u;
    auto wait_ge = [&](int f, unsigned v, bool sleep) {
#pragma unroll 1
      for (unsigned n = 0; n < SPIN_CAP; ++n) { if (dry || FL[f] >= v) break; if (sleep) __builtin_amdgcn_s_sleep(1); }
      MEMBAR();
    };
    auto arrive = [&](int f) {
      MEMBAR();
      if (lane == 0) (void)__hip_atomic_fetch_add(LU + L_FLAG + f, 1u, __ATOMIC_RELAXED, __HIP_MEMORY_SCOPE_WORKGROUP);
      MEMBAR();
    };
    auto setf = [&](int f, unsigned v) { MEMBAR(); if (lane == 0) FL[f] = v; };
    auto wait_until = [&](unsigned t0, unsigned dt) {
      while (!dry && (unsigned)__builtin_amdgcn_s_memrealtime() - t0 < dt) __builtin_amdgcn_s_sleep(1);
    };

    if (wave == 1 || wave == 6) {
      float v[9];
#pragma unroll
      for (int k = 0; k < 4; ++k) { v[2 * k] = lo16(xa[k]); v[2 * k + 1] = hi16(xa[k]); }
      v[8] = lo16(xb);
      layernorm9(v);
      store9(LU + L_XN, v, lane);
      WSTAMP(1);
      float a[8];
#pragma unroll
      for (int k = 0; k < 8; ++k) a[k] = 0.f;
#pragma unroll
      for (int i = 0; i < 9; ++i) {
        const unsigned xp = LU[L_XN + 72 * (gi & 3) + 8 * i + (lane >> 3)];
#pragma unroll
        for (int k = 0; k < 4; ++k) {
          a[2 * k] = dot2(pklo(R[2 * i][k], R[2 * i + 1][k]), xp, a[2 * k]);
          a[2 * k + 1] = dot2(pkhi(R[2 * i][k], R[2 * i + 1][k]), xp, a[2 * k + 1]);
        }
      }
      const float rr = red8(a, lane);
      const unsigned tg = __builtin_amdgcn_readfirstlane(ew) + 1u;
      const int col = 8 * (lane & 7) + ((lane >> 3) & 1) * 4 + ((lane >> 4) & 1) * 2 + (lane >> 5);
      STSC8((v2u{asu(rr), tg}), rws, (WS_Q + (u * 64 + col) * 8) | gm);
      MEMBAR();
      if (lane == 0) { FL[F_TAG] = tg; FL[F_QPUB] = 1u; }
      WSTAMP(2);
    }
    int wv2 = wave;
    asm volatile("" : "+s"(wv2));
    if (wv2 != 1 && wv2 != 6) {
      const auto rk = mk(kc + kvb, dry ? 0 : 65536);
      const int kq = dry ? 0 : kw;
      LAS unsigned* kt = LU + L_KT + kq * 4096;
      if (!dry) {
#pragma unroll
        for (int j = 8; j < 16; ++j) { VMWAIT(7); R[j] = LDNT(rk, kvo, j * 4096); }
      }
      VMWAIT(6); if (lane == 0) FL[F_K0 + kq] = 1u;
      VMWAIT(4); if (lane == 0) FL[F_K0 + kq] = 2u;
      VMWAIT(2); if (lane == 0) FL[F_K0 + kq] = 3u;
      VMWAIT(0); if (lane == 0) FL[F_K0 + kq] = 4u;
      SB();
#pragma unroll
      for (int j = 0; j < 16; ++j) *(LAS v4u*)(kt + (4 * j + r) * 64 + ((c ^ ((4 * j + r) & 15)) * 4)) = R[j];
#pragma unroll
      for (int f = 0; f < 16; ++f) R[f] = *(const LAS v4u*)(kt + (16 * (f >> 2) + c) * 64 + (((4 * (f & 3) + r) ^ c) * 4));
      PIN8(R); PIN8(R + 8);
      WSTAMP(1);
      wait_ge(F_Q, 1, false);
      WSTAMP(2);
      v4f tt[4];
#pragma unroll
      for (int T = 0; T < 4; ++T) tt[T] = v4f{0.f, 0.f, 0.f, 0.f};
#pragma unroll
      for (int k = 0; k < 4; ++k) {
        const v4u qf = *(const LAS v4u*)(LU + L_QB + (c & 1) * 64 + 16 * k + 4 * r);
#pragma unroll
        for (int T = 0; T < 4; ++T) tt[T] = mfma(qf, R[4 * T + k], tt[T]);
      }
      const float d0 = (r & 2) ? ((r & 1) ? tt[3][0] : tt[2][0]) : ((r & 1) ? tt[1][0] : tt[0][0]);
      const float d1 = (r & 2) ? ((r & 1) ? tt[3][1] : tt[2][1]) : ((r & 1) ? tt[1][1] : tt[0][1]);
      const float s0 = rbf(rbf(d0) * 0.08838834764831845f), s1 = rbf(rbf(d1) * 0.08838834764831845f);
      float m;
      {
        auto sw = __builtin_amdgcn_permlane32_swap(asu(s0), asu(s1), false, false);
        m = fmaxf(asf(sw[0]), asf(sw[1]));
        m = fmaxf(m, dpp<XOR1>(m)); m = fmaxf(m, dpp<XOR2>(m)); m = fmaxf(m, dpp<ROR4>(m)); m = fmaxf(m, dpp<XOR8>(m)); m = max16(m);
      }
      if ((lane & 31) == 0) L[L_MW + (lane >> 5) * 4 + kq] = m;
      arrive(F_MCNT);
      wait_ge(F_MCNT, 4, false);
      WSTAMP(3);
      const v4f ma = *(const LAS v4f*)(L + L_MW), mb = *(const LAS v4f*)(L + L_MW + 4);
      const float M0 = fmaxf(fmaxf(ma.x, ma.y), fmaxf(ma.z, ma.w)), M1 = fmaxf(fmaxf(mb.x, mb.y), fmaxf(mb.z, mb.w));
      const float p0 = __builtin_amdgcn_exp2f((s0 - M0) * LOG2E), p1 = __builtin_amdgcn_exp2f((s1 - M1) * LOG2E);
      const unsigned b0 = asu(rbf(p0)), b1_ = asu(rbf(p1));
      LU[L_PP + kq * 128 + (lane & 3) * 16 + (lane >> 2)] = pkhi(b0, b0);
      LU[L_PP + kq * 128 + 64 + (lane & 3) * 16 + (lane >> 2)] = pkhi(b1_, b1_);
      setf(F_P0 + kq, 1u);
      float l = tr32(p0, p1);
      l += dpp<XOR1>(l); l += dpp<XOR2>(l); l += dpp<ROR4>(l); l += dpp<XOR8>(l); l = tr16(l, l);
      if ((lane & 31) == 0) L[L_LW + (lane >> 5) * 4 + kq] = l;
      arrive(F_OCNT);
      WSTAMP(4);
    }
  }
  unsigned prog;
  if (wave == 0) prog = (B_A | B_DRY) | (B_A << 4);
  else if (wave == 1) prog = B_W;
  else if (wave < 6) prog = B_R;
  else if (wave < 8) prog = B_V | (B_R << 4);
  else if (wave < 10) prog = ((wave == 8 ? B_V : B_R) | B_DRY) | (B_V << 4) | (B_R << 8);
  else if (wave == 10) prog = (B_B | B_DRY) | (B_B << 4);
  else prog = wave == 11 ? ((B_W | B_DRY) | (B_W << 4)) : B_W;
  if (!DRYRUN && (prog & B_DRY)) prog >>= 4;

#pragma unroll 1
  for (; prog != 0u; prog >>= 4) {
    asm volatile("" : "+v"(lane), "+s"(wave), "+s"(u));
    g = u >> 4; gi = u & 15;
    const int c = lane & 15, r = lane >> 4;
    const size_t kvb = ((size_t)g * SK + (SK - W) + gi * 256) * 128;
    const int kw = (wave - 2) & 3;
    const unsigned kvoff = kw * 1024 + lane * 16;
    const int lg = (u >> 5) * 8 + (u & 7);
    const bool is_red = lg < 36;
    const int rcol = 16 * lg + 4 * ((u >> 3) & 3);
    const bool dry = (prog & B_DRY) != 0u;
    const int body = prog & 7;
    v4u R[18];
    if (dry) while ((unsigned)__builtin_amdgcn_s_memrealtime() - tk0 < TDRY) __builtin_amdgcn_s_sleep(2);
    LAS float* L = (LAS float*)smem + (dry ? L_DRY : 0);
    LAS unsigned* LU = (LAS unsigned*)L;
    volatile LAS unsigned* FL = (volatile LAS unsigned*)(LU + L_FLAG);
    const unsigned gm = dry ? OOB : 0u;
    auto wait_ge = [&](int f, unsigned v, bool sleep) {
#pragma unroll 1
      for (unsigned n = 0; n < SPIN_CAP; ++n) { if (dry || FL[f] >= v) break; if (sleep) __builtin_amdgcn_s_sleep(1); }
      MEMBAR();
    };
    auto arrive = [&](int f) {
      MEMBAR();
      if (lane == 0) (void)__hip_atomic_fetch_add(LU + L_FLAG + f, 1u, __ATOMIC_RELAXED, __HIP_MEMORY_SCOPE_WORKGROUP);
      MEMBAR();
    };
    auto setf = [&](int f, unsigned v) { MEMBAR(); if (lane == 0) FL[f] = v; };
    auto wait_until = [&](unsigned t0, unsigned dt) {
      while (!dry && (unsigned)__builtin_amdgcn_s_memrealtime() - t0 < dt) __builtin_amdgcn_s_sleep(1);
    };

    if (body == B_V) {
      const auto rv = mk(vc + kvb, 65536);
#pragma unroll
      for (int j = 0; j < 16; ++j) asm volatile("" : "=v"(R[j]));
      if (!dry) {
#pragma unroll
        for (int j = 0; j < 16; ++j) {
          if (j < 8 && (j & 1) == 0) wait_ge(F_K0 + kw, j / 2 + 1, true);
          if (j >= 8) { if ((unsigned)__builtin_amdgcn_s_memrealtime() - tk0 < TCATCH) VMWAIT(7); }
          R[j] = LDNT(rv, kvoff, j * 4096);
          SB();
        }
      }
      arrive(F_KVI);
      WSTAMP(1);
      v4u msk;
#pragma unroll
      for (int k = 0; k < 4; ++k) msk[k] = ((c & 7) >> 1) == k ? ((c & 1) ? 0xffff0000u : 0x0000ffffu) : 0u;
      wait_ge(F_P0 + kw, 1, true);
      WSTAMP(2);
      v4u pj[4];
#pragma unroll
      for (int k = 0; k < 4; ++k) pj[k] = *(const LAS v4u*)(LU + L_PP + kw * 128 + (c >> 3) * 64 + r * 16 + 4 * k);
      v4f acc = {0.f, 0.f, 0.f, 0.f}, acc2 = {0.f, 0.f, 0.f, 0.f};
#pragma unroll
      for (int j = 0; j < 16; ++j) {
        const unsigned pv_ = pj[j >> 2][j & 3];
        const v4u af = {msk[0] & pv_, msk[1] & pv_, msk[2] & pv_, msk[3] & pv_};
        if (j & 1) acc2 = mfma(af, R[j], acc2); else acc = mfma(af, R[j], acc);
      }
      acc += acc2;
      *(LAS v4f*)(L + L_OP + kw * 256 + (lane >> 5) * 128 + 8 * c + 4 * (r & 1)) = acc;
      arrive(F_OCNT);
      WSTAMP(4);
    } else if (body == B_R) {
      if (is_red || dry) {
        const int ww = (wave - 2) & 7;
        const unsigned off = (WS_P + ((32 * ww + (lane >> 1)) * 576 + rcol + 2 * (lane & 1)) * 8) | gm;
        const unsigned tag = FL[F_TAG];
        wait_ge(F_PPUB, 1, true);
        if (!dry) __builtin_amdgcn_s_sleep(DLY_P);
        WSTAMP(5);
        v4u pv = {0u, 0u, 0u, 0u};
#pragma unroll 1
        for (unsigned n = dry ? SPIN_CAP : 0; n < SPIN_CAP; ++n) {
          MEMBAR();
          pv = LDSC(rws, off, 0);
          if (__all(((pv.y ^ tag) | (pv.w ^ tag)) == 0)) break;
        }
        WSTAMP(6);
        float s0 = tr32(asf(pv.x), asf(pv.z));
        s0 += dpp<XOR2>(s0); s0 += dpp<ROR4>(s0); s0 += dpp<XOR8>(s0); s0 = tr16(s0, s0);
        if ((lane & 30) == 0) L[L_FIN + ww * 4 + 2 * (lane & 1) + (lane >> 5)] = s0;
        arrive(F_FCNT);
      }
    } else if (body == B_W) {
      const auto ro = mk(wo + (size_t)u * 16 * D, 16 * D * 2), r2 = mk(w2 + (size_t)u * 18 * D, 18 * D * 2), r1 = mk(w1, 576 * F * 2);
      const bool has1 = wave >= 11 && wave < 14;
      const unsigned tb0 = __builtin_amdgcn_readfirstlane(FL[F_T0]);
      const int cb = wave == 1 ? 512 : 128 * (wave - 11);
      const bool colok = wave != 1 || c < 8;
      const unsigned off2 = (colok ? (unsigned)(r * D + cb + 8 * c) * 2 : OOB) | gm;
      const int cl = 8 * c + 4 * (r & 1) + 2 * (r >> 1);
#pragma unroll
      for (int j = 0; j < 18; ++j) asm volatile("" : "=v"(R[j]));
      if (!dry) {
        if (has1) {
          const unsigned off1 = c < 12 ? (unsigned)((36 * gi + r) * F + 288 * g + 96 * (wave - 11) + 8 * c) * 2 : OOB;
          wait_until(tb0, TW1); wait_ge(F_KVI, 4, true);
          WSTAMP(3);
#pragma unroll
          for (int k = 0; k < 9; ++k) R[9 + k] = LDNT(r1, off1, k * (4 * F * 2));
        }
        wait_until(tb0, TWF); wait_ge(F_KVI, 4, true);
        WSTAMP(4);
#pragma unroll
        for (int k = 0; k < 5; ++k) R[4 + k] = LDNT(r2, off2 + k * (4 * D * 2), 0);
      }
      SB();
      wait_ge(F_Y, 1, true);
      const unsigned tag = FL[F_TAG];
      if (has1) {
        v4f acc = {0.f, 0.f, 0.f, 0.f};
#pragma unroll
        for (int k = 0; k < 9; ++k) acc = mfma(*(const LAS v4u*)(LU + L_FY + k * 256 + 4 * lane), R[9 + k], acc);
        const auto s0 = __builtin_amdgcn_permlane32_swap(asu(acc[0]), asu(acc[2]), false, false);
        const auto s1 = __builtin_amdgcn_permlane32_swap(asu(acc[1]), asu(acc[3]), false, false);
        STSC((v4u{s0[0], tag, s1[0], tag}), rws, (c < 12 ? WS_H + (u * 288 + 96 * (wave - 11) + cl) * 8 : OOB) | gm);
        arrive(F_HPUB);
        WSTAMP(5);
      }
      while (!dry && FL[F_POLL] < 2u && (unsigned)__builtin_amdgcn_s_memrealtime() - tb0 < TWF2) __builtin_amdgcn_s_sleep(1);
      MEMBAR();
      if (!dry) {
#pragma unroll
        for (int k = 0; k < 4; ++k) R[k] = LDNT(ro, off2, k * (4 * D * 2));
      }
      SB();
      const unsigned poff = (colok ? WS_P + (u * 576 + cb + cl) * 8 : OOB) | gm;
      v4f acc = {0.f, 0.f, 0.f, 0.f}, acc2 = {0.f, 0.f, 0.f, 0.f}, acc3 = {0.f, 0.f, 0.f, 0.f};
      wait_ge(F_CO, 2, false);
#pragma unroll
      for (int k = 0; k < 9; ++k) {
        const v4u af = *(const LAS v4u*)(LU + L_FR + k * 256 + 4 * lane);
        if (k % 3 == 0) acc = mfma(af, R[k], acc); else if (k % 3 == 1) acc2 = mfma(af, R[k], acc2); else acc3 = mfma(af, R[k], acc3);
      }
      acc += acc2 + acc3;
      const auto s0 = __builtin_amdgcn_permlane32_swap(asu(acc[0]), asu(acc[2]), false, false);
      const auto s1 = __builtin_amdgcn_permlane32_swap(asu(acc[1]), asu(acc[3]), false, false);
      STSC((v4u{s0[0], tag, s1[0], tag}), rws, poff);
      setf(F_PPUB, 1u);
      WSTAMP(7);
    } else if (body == B_B) {
      const auto rb1 = mk(b1, F * 2);
      const bool hv_ok = (lane & 31) < 18;
      unsigned b1r = 0u;
      if (!dry) b1r = __builtin_amdgcn_raw_buffer_load_b16(rb1, hv_ok ? (unsigned)(18 * u + (lane & 31)) * 2 : OOB, 0, 2);
      wait_ge(F_QPUB, 1, true);
      const unsigned tag = __builtin_amdgcn_readfirstlane(FL[F_TAG]);
      {
        const v4u ya = *(const LAS v4u*)(LU + L_XN + 4 * lane);
        const unsigned yb = ((const LAS u16*)(LU + L_XN))[512 + lane];
        float v[9];
#pragma unroll
        for (int k = 0; k < 4; ++k) { v[2 * k] = lo16(ya[k]); v[2 * k + 1] = hi16(ya[k]); }
        v[8] = lo16(yb);
        layernorm9(v);
        store9(LU + L_Y, v, lane);
#pragma unroll
        for (int it = 0; it < 5; ++it) {
          const int idx = it * 64 + lane, n = idx < 288 ? idx >> 3 : 35, m = idx & 7;
          ((LAS u16*)(LU + L_FY))[((n >> 2) * 64 + (n & 3) * 16 + m) * 8 + m] = ((const LAS u16*)(LU + L_Y))[36 * gi + n];
        }
      }
      setf(F_Y, 1u);
      WSTAMP(1);
      const unsigned hoff = (hv_ok ? WS_H + ((16 * g + (lane >> 5)) * 288 + 18 * gi + (lane & 31)) * 8 : OOB) | gm;
      const unsigned tagx = hv_ok ? tag : 0u;
      wait_ge(F_HPUB, 3, true);
      if (!dry) __builtin_amdgcn_s_sleep(DLY_H);
      wait_until(__builtin_amdgcn_readfirstlane(FL[F_T0]), TPH);
      WSTAMP(2);
      v2u hv[8];
#pragma unroll
      for (int k = 0; k < 8; ++k) hv[k] = v2u{0u, 0u};
#pragma unroll 1
      for (unsigned n = dry ? SPIN_CAP : 0; n < SPIN_CAP; ++n) {
        MEMBAR();
        unsigned bad = 0;
#pragma unroll
        for (int k = 0; k < 8; ++k) hv[k] = LDSC8(rws, hoff, k * (2 * 288 * 8));
        if (n == 0) arrive(F_POLL);
#pragma unroll
        for (int k = 0; k < 8; ++k) bad |= hv[k].y ^ tagx;
        if (__all(bad == 0)) break;
      }
      WSTAMP(3);
      float pre = ((asf(hv[0].x) + asf(hv[1].x)) + (asf(hv[2].x) + asf(hv[3].x))) + ((asf(hv[4].x) + asf(hv[5].x)) + (asf(hv[6].x) + asf(hv[7].x)));
      pre = tr32(pre, pre);
      const float tt = rbf(rbf(pre) + lo16(b1r));
      const float inner = 0.7978845608028654f * (tt + 0.044715f * tt * tt * tt);
      const float th = 1.f - 2.f * __builtin_amdgcn_rcpf(__builtin_amdgcn_exp2f(inner * (2.f * LOG2E)) + 1.f);
      const float h = rbf(0.5f * tt * (1.f + th));
      if (hv_ok) {
        const int n = 16 + (lane & 31), m0 = 4 * (lane >> 5);
        LAS u16* fp = (LAS u16*)(LU + L_FR) + ((n >> 2) * 64 + (n & 3) * 16 + m0) * 8 + m0;
        fp[0] = (u16)(asu(h) >> 16); fp[9] = (u16)(asu(h) >> 16); fp[18] = (u16)(asu(h) >> 16); fp[27] = (u16)(asu(h) >> 16);
      }
      arrive(F_CO);
      WSTAMP(4);
    } else {
      const auto rx = mk(x, D * 2), rb2 = mk(b2, D * 2);
      const unsigned xoff = (is_red ? (unsigned)(rcol + (lane & 3)) * 2 : OOB) | gm;
      unsigned xrr = 0u, b2r = 0u;
      if (!dry) { xrr = __builtin_amdgcn_raw_buffer_load_b16(rx, xoff, 0, 0); b2r = __builtin_amdgcn_raw_buffer_load_b16(rb2, xoff, 0, 0); }
      wait_ge(F_QPUB, 1, true);
      const unsigned tag = __builtin_amdgcn_readfirstlane(FL[F_TAG]);
      if (!dry) __builtin_amdgcn_s_sleep(DLY_Q);
      WSTAMP(1);
      {
        const unsigned qoff = WS_Q + g * 8192 + lane * 16;
#pragma unroll
        for (int k = 0; k < 8; ++k) R[k] = v4u{0u, 0u, 0u, 0u};
#pragma unroll 1
        for (unsigned n = dry ? SPIN_CAP : 0; n < SPIN_CAP; ++n) {
          MEMBAR();
          unsigned bad = 0;
#pragma unroll
          for (int k = 0; k < 8; ++k) R[k] = LDSC(rws, qoff, k * 1024);
#pragma unroll
          for (int k = 0; k < 8; ++k) bad |= (R[k].y ^ tag) | (R[k].w ^ tag);
          if (__all(bad == 0)) break;
        }
        float s[8];
#pragma unroll
        for (int a = 0; a < 4; ++a) { s[2 * a] = asf(R[2 * a].x) + asf(R[2 * a + 1].x); s[2 * a + 1] = asf(R[2 * a].z) + asf(R[2 * a + 1].z); }
        const float r0 = tr32(s[0], s[4]), r1_ = tr32(s[1], s[5]), r2_ = tr32(s[2], s[6]), r3 = tr32(s[3], s[7]);
        const int a0 = (lane >> 5) * 2;
        LU[L_QB + a0 * 32 + (lane & 31)] = pkhi(asu(rbf(r0)), asu(rbf(r1_)));
        LU[L_QB + (a0 + 1) * 32 + (lane & 31)] = pkhi(asu(rbf(r2_)), asu(rbf(r3)));
        setf(F_Q, 1u);
        WSTAMP(2);
      }
      wait_ge(F_OCNT, 8, false);
      WSTAMP(3);
      {
        v2f oa = *(const LAS v2f*)(L + L_OP + 2 * lane), ob = *(const LAS v2f*)(L + L_OP + 128 + 2 * lane);
#pragma unroll
        for (int w = 1; w < 4; ++w) { oa += *(const LAS v2f*)(L + L_OP + w * 256 + 2 * lane); ob += *(const LAS v2f*)(L + L_OP + w * 256 + 128 + 2 * lane); }
        const int hd = lane & 1;
        const v4f la = *(const LAS v4f*)(L + L_LW + hd * 4), ma = *(const LAS v4f*)(L + L_MW + hd * 4);
        const float Ls = (la.x + la.y) + (la.z + la.w);
        const float M = fmaxf(fmaxf(ma.x, ma.y), fmaxf(ma.z, ma.w));
        STSC((v4u{asu(oa.x), tag, asu(oa.y), tag}), rws, (WS_O + u * 2048 + lane * 16) | gm);
        STSC((v4u{asu(ob.x), tag, asu(ob.y), tag}), rws, (WS_O + u * 2048 + 1024 + lane * 16) | gm);
        STSC((v4u{asu(M), tag, asu(Ls), tag}), rws, (lane < 8 ? WS_M + u * 128 + lane * 16 : OOB) | gm);
      }
      WSTAMP(4);
      if (!dry) __builtin_amdgcn_s_sleep(DLY_O);
      wait_until(tk0, TPO);
      {
        const unsigned ooff = (WS_O + (16 * g + r) * 2048 + (16 * gi + c) * 8) | gm;
        const unsigned moff = (WS_M + (16 * g + r) * 128 + (gi >> 3) * 16) | gm;
        v2u ov[4]; v4u ml[4];
#pragma unroll
        for (int k = 0; k < 4; ++k) { ov[k] = v2u{0u, 0u}; ml[k] = v4u{0u, 0u, 0u, 0u}; }
#pragma unroll 1
        for (unsigned n = dry ? SPIN_CAP : 0; n < SPIN_CAP; ++n) {
          MEMBAR();
          unsigned bad = 0;
#pragma unroll
          for (int k = 0; k < 4; ++k) { ov[k] = LDSC8(rws, ooff, k * 8192); ml[k] = LDSC(rws, moff, k * 512); }
          if (n == 0) arrive(F_POLL);
#pragma unroll
          for (int k = 0; k < 4; ++k) bad |= (ov[k].y ^ tag) | (ml[k].y ^ tag) | (ml[k].w ^ tag);
          if (__all(bad == 0)) break;
        }
        WSTAMP(5);
        float M = fmaxf(fmaxf(asf(ml[0].x), asf(ml[1].x)), fmaxf(asf(ml[2].x), asf(ml[3].x)));
        M = max16(M); M = max32(M);
        float num = 0.f, den = 0.f;
#pragma unroll
        for (int k = 0; k < 4; ++k) { const float sc = __builtin_amdgcn_exp2f((asf(ml[k].x) - M) * LOG2E); num = fmaf(sc, asf(ov[k].x), num); den = fmaf(sc, asf(ml[k].z), den); }
        num = tr16(num, num); den = tr16(den, den); num = tr32(num, num); den = tr32(den, den);
        const float o = rbf(num * __builtin_amdgcn_rcpf(den));
        LAS u16* fp = (LAS u16*)(LU + L_FR) + ((c >> 2) * 64 + (c & 3) * 16 + 2 * r) * 8 + 2 * r;
        fp[0] = (u16)(asu(o) >> 16); fp[9] = (u16)(asu(o) >> 16);
        arrive(F_CO);
        WSTAMP(6);
      }
      if (is_red || dry) {
        const float xb2 = lo16(xrr) + lo16(b2r);
        const auto rout = mk(out, D * 2);
        wait_ge(F_FCNT, 8, false);
        float s0 = L[L_FIN + (lane & 31)];
        s0 += dpp<ROR4>(s0); s0 += dpp<XOR8>(s0); s0 = tr16(s0, s0);
        __builtin_amdgcn_raw_buffer_store_b16((u16)(asu(rbf(s0 + xb2)) >> 16), rout, lane < 4 ? xoff : OOB, 0, 0);
        __builtin_amdgcn_raw_buffer_store_b32(tag, rws, (u == 0 && lane == 0 ? WS_E : OOB) | gm, 0, 16);
        WSTAMP(7);
      }
    }
  }
}

extern "C" void kernel_launch(void* const* d_in, const int* in_sizes, int n_in,
                              void* d_out, int out_size, void* d_ws, size_t ws_size,
                              hipStream_t stream) {
  (void)hipFuncSetAttribute((const void*)fused_decode, hipFuncAttributeMaxDynamicSharedMemorySize, LDS_BYTES);
  fused_decode<<<256, NTHR, LDS_BYTES, stream>>>((const u16*)d_in[0], (const u16*)d_in[1], (const u16*)d_in[2], (const u16*)d_in[3],
                                                 (const u16*)d_in[4], (const u16*)d_in[6], (const u16*)d_in[7], (const u16*)d_in[8],
                                                 (const u16*)d_in[9], (u16*)d_out, (unsigned char*)d_ws);
}
```

```cpp
#include <hip/hip_runtime.h>
#include <hip/hip_bf16.h>
#include <cstdint>

typedef unsigned short u16;
typedef unsigned v4u __attribute__((ext_vector_type(4)));
typedef unsigned v2u __attribute__((ext_vector_type(2)));
typedef float v4f __attribute__((ext_vector_type(4)));
typedef float v2f __attribute__((ext_vector_type(2)));
typedef __bf16 bf16x2 __attribute__((ext_vector_type(2)));
typedef short bf16x8 __attribute__((ext_vector_type(8)));
#define LAS __attribute__((address_space(3)))

#define CODE_KB 14
#define KSLEEP 10
#define DLY_Q 40
#define DLY_O 24
#define DLY_H 24
#define DLY_P 30
#define TW1 540
#define TWF 600
#define TWF2 720
#define TPO 780
#define TPH 780
#define TCATCH 480

constexpr int D = 576, SK = 65536, W = 4096, F = 4608;
constexpr int NTHR = 960;
constexpr unsigned OOB = 0x80000000u;
constexpr unsigned WS_E = 0;
constexpr unsigned WS_Q = 4096;
constexpr unsigned WS_H = WS_Q + 256 * 64 * 8;
constexpr unsigned WS_O = WS_H + 256 * 288 * 8;
constexpr unsigned WS_M = WS_O + 256 * 256 * 8;
constexpr unsigned WS_P = WS_M + 256 * 128;
constexpr unsigned WS_END = WS_P + 256 * 576 * 8;
constexpr int L_XN = 0, L_Y = L_XN + 288, L_QB = L_Y + 288, L_MW = L_QB + 128, L_LW = L_MW + 16, L_OP = L_LW + 16, L_FLAG = L_OP + 4 * 256,
              L_FR = L_FLAG + 32, L_FY = L_FR + 9 * 256, L_FIN = L_FY + 9 * 256, L_PP = L_FIN + 32, L_KT = L_PP + 512, L_END = L_KT + 16384;
constexpr int LDS_BYTES = L_END * 4;
static_assert(LDS_BYTES <= 160 * 1024 && LDS_BYTES > 80 * 1024, "one workgroup per CU");
enum { F_Y = 0, F_QPUB, F_Q, F_MCNT, F_OCNT, F_HPUB, F_CO, F_PPUB, F_TAG, F_KVI, F_FCNT, F_T0, F_POLL, F_P0 = 16, F_K0 = 20 };
constexpr unsigned SPIN_CAP = 1u << 20;
constexpr unsigned POLL_CAP = 1u << 16;
constexpr float LOG2E = 1.4426950408889634f;

__device__ __forceinline__ float rbf(float f) {
  unsigned u = __float_as_uint(f);
  u = (u + 0x7fffu + ((u >> 16) & 1u)) & 0xffff0000u;
  return __uint_as_float(u);
}
__device__ __forceinline__ float lo16(unsigned w) { return __uint_as_float(w << 16); }
__device__ __forceinline__ float hi16(unsigned w) { return __uint_as_float(w & 0xffff0000u); }
__device__ __forceinline__ float asf(unsigned w) { return __uint_as_float(w); }
__device__ __forceinline__ unsigned asu(float w) { return __float_as_uint(w); }
__device__ __forceinline__ unsigned pklo(unsigned a, unsigned b) { return __builtin_amdgcn_perm(b, a, 0x05040100u); }
__device__ __forceinline__ unsigned pkhi(unsigned a, unsigned b) { return __builtin_amdgcn_perm(b, a, 0x07060302u); }
__device__ __forceinline__ float dot2(unsigned k, unsigned q, float acc) {
  return __builtin_amdgcn_fdot2_f32_bf16(__builtin_bit_cast(bf16x2, k), __builtin_bit_cast(bf16x2, q), acc, false);
}
__device__ __forceinline__ v4f mfma(v4u a, v4u b, v4f c) {
  return __builtin_amdgcn_mfma_f32_16x16x32_bf16(__builtin_bit_cast(bf16x8, a), __builtin_bit_cast(bf16x8, b), c, 0, 0, 0);
}
template <int CTRL> __device__ __forceinline__ float dpp(float x) {
  return __builtin_bit_cast(float, __builtin_amdgcn_mov_dpp(__builtin_bit_cast(int, x), CTRL, 0xf, 0xf, true));
}
constexpr int XOR1 = 0xB1, XOR2 = 0x4E, XOR8 = 0x128, ROR4 = 0x124;
__device__ __forceinline__ float tr16(float a, float b) {
  auto s = __builtin_amdgcn_permlane16_swap(asu(a), asu(b), false, false);
  return asf(s[0]) + asf(s[1]);
}
__device__ __forceinline__ float tr32(float a, float b) {
  auto s = __builtin_amdgcn_permlane32_swap(asu(a), asu(b), false, false);
  return asf(s[0]) + asf(s[1]);
}
__device__ __forceinline__ float max16(float a) {
  auto s = __builtin_amdgcn_permlane16_swap(asu(a), asu(a), false, false);
  return fmaxf(asf(s[0]), asf(s[1]));
}
__device__ __forceinline__ float max32(float a) {
  auto s = __builtin_amdgcn_permlane32_swap(asu(a), asu(a), false, false);
  return fmaxf(asf(s[0]), asf(s[1]));
}
__device__ __forceinline__ float wave_sum(float t) {
  t += dpp<XOR1>(t); t += dpp<XOR2>(t); t += dpp<ROR4>(t); t += dpp<XOR8>(t);
  t = tr16(t, t); t = tr32(t, t);
  return t;
}
__device__ __forceinline__ float red8(float (&a)[8], int lane) {
#pragma unroll
  for (int h = 0; h < 4; ++h) { const float t1 = a[h] + dpp<XOR8>(a[h]), t2 = a[h + 4] + dpp<XOR8>(a[h + 4]); a[h] = (lane & 8) ? t2 : t1; }
  a[0] = tr16(a[0], a[2]); a[1] = tr16(a[1], a[3]);
  return tr32(a[0], a[1]);
}
#define VMWAIT(n) asm volatile("s_waitcnt vmcnt(%0)" :: "n"(n) : "memory")
#define LGKM0() asm volatile("s_waitcnt lgkmcnt(0)" ::: "memory")
#define MEMBAR() asm volatile("" ::: "memory")
#define LDNT(r, off, so) __builtin_amdgcn_raw_buffer_load_b128((r), (off), (so), 2)
#define LDSC(r, off, so) __builtin_amdgcn_raw_buffer_load_b128((r), (off), (so), 16)
#define LDSC8(r, off, so) __builtin_amdgcn_raw_buffer_load_b64((r), (off), (so), 16)
#define STSC(v, r, off) __builtin_amdgcn_raw_buffer_store_b128((v), (r), (off), 0, 16)
#define STSC8(v, r, off) __builtin_amdgcn_raw_buffer_store_b64((v), (r), (off), 0, 16)
#define SB() __builtin_amdgcn_sched_barrier(0)
#define PIN8(a) asm volatile("" : "+v"((a)[0]), "+v"((a)[1]), "+v"((a)[2]), "+v"((a)[3]), "+v"((a)[4]), "+v"((a)[5]), "+v"((a)[6]), "+v"((a)[7]))

__device__ __forceinline__ void layernorm9(float (&v)[9]) {
  float s = ((v[0] + v[1]) + (v[2] + v[3])) + ((v[4] + v[5]) + (v[6] + v[7])) + v[8];
  const float mean = wave_sum(s) * (1.f / D);
  float s2 = 0.f, s3 = 0.f;
#pragma unroll
  for (int k = 0; k < 8; k += 2) { const float d = v[k] - mean, e = v[k + 1] - mean; s2 = fmaf(d, d, s2); s3 = fmaf(e, e, s3); }
  { const float d = v[8] - mean; s2 = fmaf(d, d, s2); }
  const float var = rbf(wave_sum(s2 + s3) * (1.f / D));
  const float mb = rbf(mean);
  const float den = rbf(__builtin_amdgcn_sqrtf(rbf(var + 1e-5f)));
  const float inv = __builtin_amdgcn_rcpf(den);
#pragma unroll
  for (int k = 0; k < 9; ++k) {
    const float n = rbf(v[k] - mb);
    float qv = n * inv;
    qv = fmaf(fmaf(-qv, den, n), inv, qv);
    v[k] = rbf(qv);
  }
}
__device__ __forceinline__ void store9(LAS unsigned* dst, const float (&v)[9], int lane) {
  *(LAS v4u*)(dst + 4 * lane) = v4u{pkhi(asu(v[0]), asu(v[1])), pkhi(asu(v[2]), asu(v[3])), pkhi(asu(v[4]), asu(v[5])), pkhi(asu(v[6]), asu(v[7]))};
  ((LAS u16*)dst)[512 + lane] = (u16)(asu(v[8]) >> 16);
}

__global__ void __launch_bounds__(NTHR, 4) fused_decode(const u16* __restrict__ x, const u16* __restrict__ kc, const u16* __restrict__ vc,
                                                     const u16* __restrict__ wq, const u16* __restrict__ wo, const u16* __restrict__ w1,
                                                     const u16* __restrict__ b1, const u16* __restrict__ w2, const u16* __restrict__ b2,
                                                     u16* __restrict__ out, unsigned char* ws) {
  const unsigned tk0 = (unsigned)__builtin_amdgcn_s_memrealtime();
  unsigned long long pc0;
  asm volatile("s_getpc_b64 %0" : "=s"(pc0));
  extern __shared__ __attribute__((aligned(16))) float smem[];
  const int t = threadIdx.x;
  int lane = t & 63;
  int wave = __builtin_amdgcn_readfirstlane(t >> 6);
  int u = blockIdx.x;
  int g = u >> 4, gi = u & 15;
  auto mk = [](const void* p, unsigned bytes) { return __builtin_amdgcn_make_buffer_rsrc((void*)p, 0, bytes, 0x00020000); };
  const auto rws = mk(ws, WS_END);
  v4u R[18];
  v4u xa; unsigned xb, ew;
#pragma unroll
  for (int i = 0; i < 18; ++i) asm volatile("" : "=v"(R[i]));
  asm volatile("" : "=v"(xa), "=v"(xb), "=v"(ew));
  if (wave == 1) {
    const auto rx = mk(x, D * 2), rq = mk(wq, 576 * 4096 * 2);
    xa = __builtin_amdgcn_raw_buffer_load_b128(rx, lane * 16, 0, 0);
    xb = __builtin_amdgcn_raw_buffer_load_b16(rx, 1024 + lane * 2, 0, 0);
    ew = __builtin_amdgcn_raw_buffer_load_b32(rws, WS_E, 0, 16);
    const unsigned offq = ((144 * (gi & 3) + 2 * (lane >> 3)) * 4096 + 256 * g + 64 * (gi >> 2) + 8 * (lane & 7)) * 2;
#pragma unroll
    for (int i = 0; i < 18; ++i) R[i] = LDNT(rq, offq, ((i >> 1) * 16 + (i & 1)) * 8192);
  }
  int wv0 = wave;
  asm volatile("" : "+s"(wv0));
  if (wv0 == 0) {
    const auto rc = mk((const void*)(pc0 & ~255ull), CODE_KB * 1024);
    R[0] = __builtin_amdgcn_raw_buffer_load_b128(rc, ((u >> 3) % CODE_KB) * 1024 + lane * 16, 0, 0);
    if (lane < 32) ((LAS unsigned*)smem)[L_FLAG + lane] = lane == F_T0 ? tk0 : 0u;
  }
  asm volatile("" : "+s"(wv0));
  if (wv0 >= 10) {
#pragma unroll
    for (int k = 0; k < 4; ++k) *(LAS v4u*)((LAS unsigned*)smem + L_FR + 4 * ((k * 5 + (wv0 - 10)) * 64 + lane)) = v4u{0u, 0u, 0u, 0u};
  }
  unsigned kvo = ((wave - 2) & 3) * 1024 + lane * 16;
  asm volatile("" : "+v"(kvo));
  LGKM0();
  SB(); __builtin_amdgcn_s_barrier(); SB();
  if (wave >= 2 && wave < 6) {
    const auto rk = mk(kc + ((size_t)g * SK + (SK - W) + gi * 256) * 128, 65536);
    __builtin_amdgcn_s_sleep(KSLEEP);
#pragma unroll
    for (int j = 0; j < 8; ++j) R[j] = LDNT(rk, kvo, j * 4096);
  }
  SB();
  const int c = lane & 15, r = lane >> 4;
  const size_t kvb = ((size_t)g * SK + (SK - W) + gi * 256) * 128;
  const int kw = (wave - 2) & 3;
  const unsigned kvoff = kvo;
  const int lg = (u >> 5) * 8 + (u & 7);
  const bool is_red = lg < 36;
  const int rcol = 16 * lg + 4 * ((u >> 3) & 3);
  LAS float* L = (LAS float*)smem;
  LAS unsigned* LU = (LAS unsigned*)smem;
  volatile LAS unsigned* FL = (volatile LAS unsigned*)(LU + L_FLAG);
  auto wait_ge = [&](int f, unsigned v, bool sleep) {
#pragma unroll 1
    for (unsigned n = 0; n < SPIN_CAP; ++n) { if (FL[f] >= v) break; if (sleep) __builtin_amdgcn_s_sleep(1); }
    MEMBAR();
  };
  auto arrive = [&](int f) {
    MEMBAR();
    if (lane == 0) (void)__hip_atomic_fetch_add(LU + L_FLAG + f, 1u, __ATOMIC_RELAXED, __HIP_MEMORY_SCOPE_WORKGROUP);
    MEMBAR();
  };
  auto setf = [&](int f, unsigned v) { MEMBAR(); if (lane == 0) FL[f] = v; };
  auto wait_until = [&](unsigned t0, unsigned dt) {
    while ((unsigned)__builtin_amdgcn_s_memrealtime() - t0 < dt) __builtin_amdgcn_s_sleep(1);
  };
  if (wave == 1) {
      float v[9];
#pragma unroll
      for (int k = 0; k < 4; ++k) { v[2 * k] = lo16(xa[k]); v[2 * k + 1] = hi16(xa[k]); }
      v[8] = lo16(xb);
      layernorm9(v);
      store9(LU + L_XN, v, lane);
      float a[8];
#pragma unroll
      for (int k = 0; k < 8; ++k) a[k] = 0.f;
#pragma unroll
      for (int i = 0; i < 9; ++i) {
        const unsigned xp = LU[L_XN + 72 * (gi & 3) + 8 * i + (lane >> 3)];
#pragma unroll
        for (int k = 0; k < 4; ++k) {
          a[2 * k] = dot2(pklo(R[2 * i][k], R[2 * i + 1][k]), xp, a[2 * k]);
          a[2 * k + 1] = dot2(pkhi(R[2 * i][k], R[2 * i + 1][k]), xp, a[2 * k + 1]);
        }
      }
      const float rr = red8(a, lane);
      const unsigned tg = __builtin_amdgcn_readfirstlane(ew) + 1u;
      const int col = 8 * (lane & 7) + ((lane >> 3) & 1) * 4 + ((lane >> 4) & 1) * 2 + (lane >> 5);
      STSC8((v2u{asu(rr), tg}), rws, (WS_Q + (u * 64 + col) * 8));
      MEMBAR();
      if (lane == 0) { FL[F_TAG] = tg; FL[F_QPUB] = 1u; }
  }
  asm volatile("" : "+s"(wv0));
  if (wv0 >= 2 && wv0 < 6) {
      const auto rk = mk(kc + kvb, 65536);
      const int kq = kw;
      LAS unsigned* kt = LU + L_KT + kq * 4096;
      {
#pragma unroll
        for (int j = 8; j < 16; ++j) { VMWAIT(7); R[j] = LDNT(rk, kvo, j * 4096); }
      }
      VMWAIT(6); if (lane == 0) FL[F_K0 + kq] = 1u;
      VMWAIT(4); if (lane == 0) FL[F_K0 + kq] = 2u;
      VMWAIT(2); if (lane == 0) FL[F_K0 + kq] = 3u;
      VMWAIT(0); if (lane == 0) FL[F_K0 + kq] = 4u;
      SB();
#pragma unroll
      for (int j = 0; j < 16; ++j) *(LAS v4u*)(kt + (4 * j + r) * 64 + ((c ^ ((4 * j + r) & 15)) * 4)) = R[j];
#pragma unroll
      for (int f = 0; f < 16; ++f) R[f] = *(const LAS v4u*)(kt + (16 * (f >> 2) + c) * 64 + (((4 * (f & 3) + r) ^ c) * 4));
      PIN8(R); PIN8(R + 8);
      wait_ge(F_Q, 1, false);
      v4f tt[4];
#pragma unroll
      for (int T = 0; T < 4; ++T) tt[T] = v4f{0.f, 0.f, 0.f, 0.f};
#pragma unroll
      for (int k = 0; k < 4; ++k) {
        const v4u qf = *(const LAS v4u*)(LU + L_QB + (c & 1) * 64 + 16 * k + 4 * r);
#pragma unroll
        for (int T = 0; T < 4; ++T) tt[T] = mfma(qf, R[4 * T + k], tt[T]);
      }
      const float d0 = (r & 2) ? ((r & 1) ? tt[3][0] : tt[2][0]) : ((r & 1) ? tt[1][0] : tt[0][0]);
      const float d1 = (r & 2) ? ((r & 1) ? tt[3][1] : tt[2][1]) : ((r & 1) ? tt[1][1] : tt[0][1]);
      const float s0 = rbf(rbf(d0) * 0.08838834764831845f), s1 = rbf(rbf(d1) * 0.08838834764831845f);
      float m;
      {
        auto sw = __builtin_amdgcn_permlane32_swap(asu(s0), asu(s1), false, false);
        m = fmaxf(asf(sw[0]), asf(sw[1]));
        m = fmaxf(m, dpp<XOR1>(m)); m = fmaxf(m, dpp<XOR2>(m)); m = fmaxf(m, dpp<ROR4>(m)); m = fmaxf(m, dpp<XOR8>(m)); m = max16(m);
      }
      if ((lane & 31) == 0) L[L_MW + (lane >> 5) * 4 + kq] = m;
      arrive(F_MCNT);
      wait_ge(F_MCNT, 4, false);
      const v4f ma = *(const LAS v4f*)(L + L_MW), mb = *(const LAS v4f*)(L + L_MW + 4);
      const float M0 = fmaxf(fmaxf(ma.x, ma.y), fmaxf(ma.z, ma.w)), M1 = fmaxf(fmaxf(mb.x, mb.y), fmaxf(mb.z, mb.w));
      const float p0 = __builtin_amdgcn_exp2f((s0 - M0) * LOG2E), p1 = __builtin_amdgcn_exp2f((s1 - M1) * LOG2E);
      const unsigned b0 = asu(rbf(p0)), b1_ = asu(rbf(p1));
      LU[L_PP + kq * 128 + (lane & 3) * 16 + (lane >> 2)] = pkhi(b0, b0);
      LU[L_PP + kq * 128 + 64 + (lane & 3) * 16 + (lane >> 2)] = pkhi(b1_, b1_);
      setf(F_P0 + kq, 1u);
      float l = tr32(p0, p1);
      l += dpp<XOR1>(l); l += dpp<XOR2>(l); l += dpp<ROR4>(l); l += dpp<XOR8>(l); l = tr16(l, l);
      if ((lane & 31) == 0) L[L_LW + (lane >> 5) * 4 + kq] = l;
      arrive(F_OCNT);
  }
  asm volatile("" : "+s"(wv0));
  if (wv0 >= 2 && wv0 < 10) {
    if (wv0 >= 6) {
      v4u R[16];
      const auto rv = mk(vc + kvb, 65536);
      {
#pragma unroll
        for (int j = 0; j < 16; ++j) {
          if (j < 8 && (j & 1) == 0) wait_ge(F_K0 + kw, j / 2 + 1, true);
          if (j >= 8) { if ((unsigned)__builtin_amdgcn_s_memrealtime() - tk0 < TCATCH) VMWAIT(7); }
          R[j] = LDNT(rv, kvoff, j * 4096);
          SB();
        }
      }
      arrive(F_KVI);
      v4u msk;
#pragma unroll
      for (int k = 0; k < 4; ++k) msk[k] = ((c & 7) >> 1) == k ? ((c & 1) ? 0xffff0000u : 0x0000ffffu) : 0u;
      wait_ge(F_P0 + kw, 1, true);
      v4u pj[4];
#pragma unroll
      for (int k = 0; k < 4; ++k) pj[k] = *(const LAS v4u*)(LU + L_PP + kw * 128 + (c >> 3) * 64 + r * 16 + 4 * k);
      v4f acc = {0.f, 0.f, 0.f, 0.f}, acc2 = {0.f, 0.f, 0.f, 0.f};
#pragma unroll
      for (int j = 0; j < 16; ++j) {
        const unsigned pv_ = pj[j >> 2][j & 3];
        const v4u af = {msk[0] & pv_, msk[1] & pv_, msk[2] & pv_, msk[3] & pv_};
        if (j & 1) acc2 = mfma(af, R[j], acc2); else acc = mfma(af, R[j], acc);
      }
      acc += acc2;
      *(LAS v4f*)(L + L_OP + kw * 256 + (lane >> 5) * 128 + 8 * c + 4 * (r & 1)) = acc;
      arrive(F_OCNT);
    }
    {
      if (is_red) {
        const int ww = (wave - 2) & 7;
        const unsigned off = (WS_P + ((32 * ww + (lane >> 1)) * 576 + rcol + 2 * (lane & 1)) * 8);
        const unsigned tag = FL[F_TAG];
        wait_ge(F_PPUB, 1, true);
        __builtin_amdgcn_s_sleep(DLY_P);
        v4u pv = {0u, 0u, 0u, 0u};
#pragma unroll 1
        for (unsigned n = 0; n < POLL_CAP; ++n) {
          MEMBAR();
          pv = LDSC(rws, off, 0);
          if (__all(((pv.y ^ tag) | (pv.w ^ tag)) == 0)) break;
        }
        float s0 = tr32(asf(pv.x), asf(pv.z));
        s0 += dpp<XOR2>(s0); s0 += dpp<ROR4>(s0); s0 += dpp<XOR8>(s0); s0 = tr16(s0, s0);
        if ((lane & 30) == 0) L[L_FIN + ww * 4 + 2 * (lane & 1) + (lane >> 5)] = s0;
        arrive(F_FCNT);
      }
    }
    return;
  }
  if (wv0 == 0) {
    v4u R[8];
      const auto rx = mk(x, D * 2), rb2 = mk(b2, D * 2);
      const unsigned xoff = (is_red ? (unsigned)(rcol + (lane & 3)) * 2 : OOB);
      unsigned xrr = 0u, b2r = 0u;
      { xrr = __builtin_amdgcn_raw_buffer_load_b16(rx, xoff, 0, 0); b2r = __builtin_amdgcn_raw_buffer_load_b16(rb2, xoff, 0, 0); }
      wait_ge(F_QPUB, 1, true);
      const unsigned tag = __builtin_amdgcn_readfirstlane(FL[F_TAG]);
      __builtin_amdgcn_s_sleep(DLY_Q);
      {
        const unsigned qoff = WS_Q + g * 8192 + lane * 16;
#pragma unroll
        for (int k = 0; k < 8; ++k) R[k] = v4u{0u, 0u, 0u, 0u};
#pragma unroll 1
        for (unsigned n = 0; n < POLL_CAP; ++n) {
          MEMBAR();
          unsigned bad = 0;
#pragma unroll
          for (int k = 0; k < 8; ++k) R[k] = LDSC(rws, qoff, k * 1024);
#pragma unroll
          for (int k = 0; k < 8; ++k) bad |= (R[k].y ^ tag) | (R[k].w ^ tag);
          if (__all(bad == 0)) break;
        }
        float s[8];
#pragma unroll
        for (int a = 0; a < 4; ++a) { s[2 * a] = asf(R[2 * a].x) + asf(R[2 * a + 1].x); s[2 * a + 1] = asf(R[2 * a].z) + asf(R[2 * a + 1].z); }
        const float r0 = tr32(s[0], s[4]), r1_ = tr32(s[1], s[5]), r2_ = tr32(s[2], s[6]), r3 = tr32(s[3], s[7]);
        const int a0 = (lane >> 5) * 2;
        LU[L_QB + a0 * 32 + (lane & 31)] = pkhi(asu(rbf(r0)), asu(rbf(r1_)));
        LU[L_QB + (a0 + 1) * 32 + (lane & 31)] = pkhi(asu(rbf(r2_)), asu(rbf(r3)));
        setf(F_Q, 1u);
      }
      wait_ge(F_OCNT, 8, false);
      {
        v2f oa = *(const LAS v2f*)(L + L_OP + 2 * lane), ob = *(const LAS v2f*)(L + L_OP + 128 + 2 * lane);
#pragma unroll
        for (int w = 1; w < 4; ++w) { oa += *(const LAS v2f*)(L + L_OP + w * 256 + 2 * lane); ob += *(const LAS v2f*)(L + L_OP + w * 256 + 128 + 2 * lane); }
        const int hd = lane & 1;
        const v4f la = *(const LAS v4f*)(L + L_LW + hd * 4), ma = *(const LAS v4f*)(L + L_MW + hd * 4);
        const float Ls = (la.x + la.y) + (la.z + la.w);
        const float M = fmaxf(fmaxf(ma.x, ma.y), fmaxf(ma.z, ma.w));
        STSC((v4u{asu(oa.x), tag, asu(oa.y), tag}), rws, (WS_O + u * 2048 + lane * 16));
        STSC((v4u{asu(ob.x), tag, asu(ob.y), tag}), rws, (WS_O + u * 2048 + 1024 + lane * 16));
        STSC((v4u{asu(M), tag, asu(Ls), tag}), rws, (lane < 8 ? WS_M + u * 128 + lane * 16 : OOB));
      }
      __builtin_amdgcn_s_sleep(DLY_O);
      wait_until(tk0, TPO);
      {
        const unsigned ooff = (WS_O + (16 * g + r) * 2048 + (16 * gi + c) * 8);
        const unsigned moff = (WS_M + (16 * g + r) * 128 + (gi >> 3) * 16);
        v2u ov[4]; v4u ml[4];
#pragma unroll
        for (int k = 0; k < 4; ++k) { ov[k] = v2u{0u, 0u}; ml[k] = v4u{0u, 0u, 0u, 0u}; }
#pragma unroll 1
        for (unsigned n = 0; n < POLL_CAP; ++n) {
          MEMBAR();
          unsigned bad = 0;
#pragma unroll
          for (int k = 0; k < 4; ++k) { ov[k] = LDSC8(rws, ooff, k * 8192); ml[k] = LDSC(rws, moff, k * 512); }
          if (n == 0) arrive(F_POLL);
#pragma unroll
          for (int k = 0; k < 4; ++k) bad |= (ov[k].y ^ tag) | (ml[k].y ^ tag) | (ml[k].w ^ tag);
          if (__all(bad == 0)) break;
        }
        float M = fmaxf(fmaxf(asf(ml[0].x), asf(ml[1].x)), fmaxf(asf(ml[2].x), asf(ml[3].x)));
        M = max16(M); M = max32(M);
        float num = 0.f, den = 0.f;
#pragma unroll
        for (int k = 0; k < 4; ++k) { const float sc = __builtin_amdgcn_exp2f((asf(ml[k].x) - M) * LOG2E); num = fmaf(sc, asf(ov[k].x), num); den = fmaf(sc, asf(ml[k].z), den); }
        num = tr16(num, num); den = tr16(den, den); num = tr32(num, num); den = tr32(den, den);
        const float o = rbf(num * __builtin_amdgcn_rcpf(den));
        LAS u16* fp = (LAS u16*)(LU + L_FR) + ((c >> 2) * 64 + (c & 3) * 16 + 2 * r) * 8 + 2 * r;
        fp[0] = (u16)(asu(o) >> 16); fp[9] = (u16)(asu(o) >> 16);
        arrive(F_CO);
      }
      if (is_red) {
        const float xb2 = lo16(xrr) + lo16(b2r);
        const auto rout = mk(out, D * 2);
        wait_ge(F_FCNT, 8, false);
        float s0 = L[L_FIN + (lane & 31)];
        s0 += dpp<ROR4>(s0); s0 += dpp<XOR8>(s0); s0 = tr16(s0, s0);
        __builtin_amdgcn_raw_buffer_store_b16((u16)(asu(rbf(s0 + xb2)) >> 16), rout, lane < 4 ? xoff : OOB, 0, 0);
        __builtin_amdgcn_raw_buffer_store_b32(tag, rws, (u == 0 && lane == 0 ? WS_E : OOB), 0, 16);
      }
    return;
  }
  if (wv0 == 10) {
      const auto rb1 = mk(b1, F * 2);
      const bool hv_ok = (lane & 31) < 18;
      unsigned b1r = 0u;
      b1r = __builtin_amdgcn_raw_buffer_load_b16(rb1, hv_ok ? (unsigned)(18 * u + (lane & 31)) * 2 : OOB, 0, 2);
      wait_ge(F_QPUB, 1, true);
      const unsigned tag = __builtin_amdgcn_readfirstlane(FL[F_TAG]);
      {
        const v4u ya = *(const LAS v4u*)(LU + L_XN + 4 * lane);
        const unsigned yb = ((const LAS u16*)(LU + L_XN))[512 + lane];
        float v[9];
#pragma unroll
        for (int k = 0; k < 4; ++k) { v[2 * k] = lo16(ya[k]); v[2 * k + 1] = hi16(ya[k]); }
        v[8] = lo16(yb);
        layernorm9(v);
        store9(LU + L_Y, v, lane);
#pragma unroll
        for (int it = 0; it < 5; ++it) {
          const int idx = it * 64 + lane, n = idx < 288 ? idx >> 3 : 35, m = idx & 7;
          ((LAS u16*)(LU + L_FY))[((n >> 2) * 64 + (n & 3) * 16 + m) * 8 + m] = ((const LAS u16*)(LU + L_Y))[36 * gi + n];
        }
      }
      setf(F_Y, 1u);
      const unsigned hoff = (hv_ok ? WS_H + ((16 * g + (lane >> 5)) * 288 + 18 * gi + (lane & 31)) * 8 : OOB);
      const unsigned tagx = hv_ok ? tag : 0u;
      wait_ge(F_HPUB, 3, true);
      __builtin_amdgcn_s_sleep(DLY_H);
      wait_until(__builtin_amdgcn_readfirstlane(FL[F_T0]), TPH);
      v2u hv[8];
#pragma unroll
      for (int k = 0; k < 8; ++k) hv[k] = v2u{0u, 0u};
#pragma unroll 1
      for (unsigned n = 0; n < POLL_CAP; ++n) {
        MEMBAR();
        unsigned bad = 0;
#pragma unroll
        for (int k = 0; k < 8; ++k) hv[k] = LDSC8(rws, hoff, k * (2 * 288 * 8));
        if (n == 0) arrive(F_POLL);
#pragma unroll
        for (int k = 0; k < 8; ++k) bad |= hv[k].y ^ tagx;
        if (__all(bad == 0)) break;
      }
      float pre = ((asf(hv[0].x) + asf(hv[1].x)) + (asf(hv[2].x) + asf(hv[3].x))) + ((asf(hv[4].x) + asf(hv[5].x)) + (asf(hv[6].x) + asf(hv[7].x)));
      pre = tr32(pre, pre);
      const float tt = rbf(rbf(pre) + lo16(b1r));
      const float inner = 0.7978845608028654f * (tt + 0.044715f * tt * tt * tt);
      const float th = 1.f - 2.f * __builtin_amdgcn_rcpf(__builtin_amdgcn_exp2f(inner * (2.f * LOG2E)) + 1.f);
      const float h = rbf(0.5f * tt * (1.f + th));
      if (hv_ok) {
        const int n = 16 + (lane & 31), m0 = 4 * (lane >> 5);
        LAS u16* fp = (LAS u16*)(LU + L_FR) + ((n >> 2) * 64 + (n & 3) * 16 + m0) * 8 + m0;
        fp[0] = (u16)(asu(h) >> 16); fp[9] = (u16)(asu(h) >> 16); fp[18] = (u16)(asu(h) >> 16); fp[27] = (u16)(asu(h) >> 16);
      }
      arrive(F_CO);
    return;
  }
  {
    v4u R[18];
      const auto ro = mk(wo + (size_t)u * 16 * D, 16 * D * 2), r2 = mk(w2 + (size_t)u * 18 * D, 18 * D * 2), r1 = mk(w1, 576 * F * 2);
      const bool has1 = wave >= 11 && wave < 14;
      const unsigned tb0 = __builtin_amdgcn_readfirstlane(FL[F_T0]);
      const int cb = wave == 1 ? 512 : 128 * (wave - 11);
      const bool colok = wave != 1 || c < 8;
      const unsigned off2 = (colok ? (unsigned)(r * D + cb + 8 * c) * 2 : OOB);
      const int cl = 8 * c + 4 * (r & 1) + 2 * (r >> 1);
      {
        if (has1) {
          const unsigned off1 = c < 12 ? (unsigned)((36 * gi + r) * F + 288 * g + 96 * (wave - 11) + 8 * c) * 2 : OOB;
          wait_until(tb0, TW1); wait_ge(F_KVI, 4, true);
#pragma unroll
          for (int k = 0; k < 9; ++k) R[9 + k] = LDNT(r1, off1, k * (4 * F * 2));
        }
        wait_until(tb0, TWF); wait_ge(F_KVI, 4, true);
#pragma unroll
        for (int k = 0; k < 5; ++k) R[4 + k] = LDNT(r2, off2 + k * (4 * D * 2), 0);
      }
      SB();
      wait_ge(F_Y, 1, true);
      const unsigned tag = FL[F_TAG];
      if (has1) {
        v4f acc = {0.f, 0.f, 0.f, 0.f};
#pragma unroll
        for (int k = 0; k < 9; ++k) acc = mfma(*(const LAS v4u*)(LU + L_FY + k * 256 + 4 * lane), R[9 + k], acc);
        const auto s0 = __builtin_amdgcn_permlane32_swap(asu(acc[0]), asu(acc[2]), false, false);
        const auto s1 = __builtin_amdgcn_permlane32_swap(asu(acc[1]), asu(acc[3]), false, false);
        STSC((v4u{s0[0], tag, s1[0], tag}), rws, (c < 12 ? WS_H + (u * 288 + 96 * (wave - 11) + cl) * 8 : OOB));
        arrive(F_HPUB);
      }
      while (FL[F_POLL] < 2u && (unsigned)__builtin_amdgcn_s_memrealtime() - tb0 < TWF2) __builtin_amdgcn_s_sleep(1);
      MEMBAR();
      {
#pragma unroll
        for (int k = 0; k < 4; ++k) R[k] = LDNT(ro, off2, k * (4 * D * 2));
      }
      SB();
      const unsigned poff = (colok ? WS_P + (u * 576 + cb + cl) * 8 : OOB);
      v4f acc = {0.f, 0.f, 0.f, 0.f}, acc2 = {0.f, 0.f, 0.f, 0.f}, acc3 = {0.f, 0.f, 0.f, 0.f};
      wait_ge(F_CO, 2, false);
#pragma unroll
      for (int k = 0; k < 9; ++k) {
        const v4u af = *(const LAS v4u*)(LU + L_FR + k * 256 + 4 * lane);
        if (k % 3 == 0) acc = mfma(af, R[k], acc); else if (k % 3 == 1) acc2 = mfma(af, R[k], acc2); else acc3 = mfma(af, R[k], acc3);
      }
      acc += acc2 + acc3;
      const auto s0 = __builtin_amdgcn_permlane32_swap(asu(acc[0]), asu(acc[2]), false, false);
      const auto s1 = __builtin_amdgcn_permlane32_swap(asu(acc[1]), asu(acc[3]), false, false);
      STSC((v4u{s0[0], tag, s1[0], tag}), rws, poff);
      setf(F_PPUB, 1u);
  }
}

extern "C" void kernel_launch(void* const* d_in, const int* in_sizes, int n_in,
                              void* d_out, int out_size, void* d_ws, size_t ws_size,
                              hipStream_t stream) {
  (void)hipFuncSetAttribute((const void*)fused_decode, hipFuncAttributeMaxDynamicSharedMemorySize, LDS_BYTES);
  fused_decode<<<256, NTHR, LDS_BYTES, stream>>>((const u16*)d_in[0], (const u16*)d_in[1], (const u16*)d_in[2], (const u16*)d_in[3],
                                                 (const u16*)d_in[4], (const u16*)d_in[6], (const u16*)d_in[7], (const u16*)d_in[8],
                                                 (const u16*)d_in[9], (u16*)d_out, (unsigned char*)d_ws);
}
```

```cpp
#include <hip/hip_runtime.h>
#include <hip/hip_bf16.h>
#include <cstdint>

typedef unsigned short u16;
typedef unsigned v4u __attribute__((ext_vector_type(4)));
typedef unsigned v2u __attribute__((ext_vector_type(2)));
typedef float v4f __attribute__((ext_vector_type(4)));
typedef float v2f __attribute__((ext_vector_type(2)));
typedef __bf16 bf16x2 __attribute__((ext_vector_type(2)));
typedef short bf16x8 __attribute__((ext_vector_type(8)));
#define LAS __attribute__((address_space(3)))

#define CODE_KB 14
#define KSLEEP 10
#define DLY_Q 40
#define DLY_O 24
#define DLY_H 24
#define DLY_P 30
#define TW1 560
#define TWF 620
#define TWF2 800
#define TPO 740
#define TPH 740
#define TCATCH 480

constexpr int D = 576, SK = 65536, W = 4096, F = 4608;
constexpr int NTHR = 960;
constexpr unsigned OOB = 0x80000000u;
constexpr unsigned WS_E = 0;
constexpr unsigned WS_Q = 4096;
constexpr unsigned WS_H = WS_Q + 256 * 64 * 8;
constexpr unsigned WS_O = WS_H + 256 * 288 * 8;
constexpr unsigned WS_M = WS_O + 256 * 256 * 8;
constexpr unsigned WS_P = WS_M + 256 * 128;
constexpr unsigned WS_END = WS_P + 256 * 576 * 8;
constexpr unsigned WS_DBG = 8u << 20;
constexpr int L_XN = 0, L_Y = L_XN + 288, L_QB = L_Y + 288, L_MW = L_QB + 128, L_LW = L_MW + 16, L_OP = L_LW + 16, L_FLAG = L_OP + 4 * 256,
              L_FR = L_FLAG + 32, L_FY = L_FR + 9 * 256, L_FIN = L_FY + 9 * 256, L_PP = L_FIN + 32, L_KT = L_PP + 512, L_END = L_KT + 16384;
constexpr int LDS_BYTES = L_END * 4;
static_assert(LDS_BYTES <= 160 * 1024 && LDS_BYTES > 80 * 1024, "one workgroup per CU");
enum { F_Y = 0, F_QPUB, F_Q, F_MCNT, F_OCNT, F_HPUB, F_CO, F_PPUB, F_TAG, F_KVI, F_FCNT, F_T0, F_POLL, F_P0 = 16, F_K0 = 20 };
constexpr unsigned SPIN_CAP = 1u << 20;
constexpr unsigned POLL_CAP = 1u << 16;
constexpr float LOG2E = 1.4426950408889634f;

__device__ __forceinline__ float rbf(float f) {
  unsigned u = __float_as_uint(f);
  u = (u + 0x7fffu + ((u >> 16) & 1u)) & 0xffff0000u;
  return __uint_as_float(u);
}
__device__ __forceinline__ float lo16(unsigned w) { return __uint_as_float(w << 16); }
__device__ __forceinline__ float hi16(unsigned w) { return __uint_as_float(w & 0xffff0000u); }
__device__ __forceinline__ float asf(unsigned w) { return __uint_as_float(w); }
__device__ __forceinline__ unsigned asu(float w) { return __float_as_uint(w); }
__device__ __forceinline__ unsigned pklo(unsigned a, unsigned b) { return __builtin_amdgcn_perm(b, a, 0x05040100u); }
__device__ __forceinline__ unsigned pkhi(unsigned a, unsigned b) { return __builtin_amdgcn_perm(b, a, 0x07060302u); }
__device__ __forceinline__ float dot2(unsigned k, unsigned q, float acc) {
  return __builtin_amdgcn_fdot2_f32_bf16(__builtin_bit_cast(bf16x2, k), __builtin_bit_cast(bf16x2, q), acc, false);
}
__device__ __forceinline__ v4f mfma(v4u a, v4u b, v4f c) {
  return __builtin_amdgcn_mfma_f32_16x16x32_bf16(__builtin_bit_cast(bf16x8, a), __builtin_bit_cast(bf16x8, b), c, 0, 0, 0);
}
template <int CTRL> __device__ __forceinline__ float dpp(float x) {
  return __builtin_bit_cast(float, __builtin_amdgcn_mov_dpp(__builtin_bit_cast(int, x), CTRL, 0xf, 0xf, true));
}
constexpr int XOR1 = 0xB1, XOR2 = 0x4E, XOR8 = 0x128, ROR4 = 0x124;
__device__ __forceinline__ float tr16(float a, float b) {
  auto s = __builtin_amdgcn_permlane16_swap(asu(a), asu(b), false, false);
  return asf(s[0]) + asf(s[1]);
}
__device__ __forceinline__ float tr32(float a, float b) {
  auto s = __builtin_amdgcn_permlane32_swap(asu(a), asu(b), false, false);
  return asf(s[0]) + asf(s[1]);
}
__device__ __forceinline__ float max16(float a) {
  auto s = __builtin_amdgcn_permlane16_swap(asu(a), asu(a), false, false);
  return fmaxf(asf(s[0]), asf(s[1]));
}
__device__ __forceinline__ float max32(float a) {
  auto s = __builtin_amdgcn_permlane32_swap(asu(a), asu(a), false, false);
  return fmaxf(asf(s[0]), asf(s[1]));
}
__device__ __forceinline__ float wave_sum(float t) {
  t += dpp<XOR1>(t); t += dpp<XOR2>(t); t += dpp<ROR4>(t); t += dpp<XOR8>(t);
  t = tr16(t, t); t = tr32(t, t);
  return t;
}
__device__ __forceinline__ float red8(float (&a)[8], int lane) {
#pragma unroll
  for (int h = 0; h < 4; ++h) { const float t1 = a[h] + dpp<XOR8>(a[h]), t2 = a[h + 4] + dpp<XOR8>(a[h + 4]); a[h] = (lane & 8) ? t2 : t1; }
  a[0] = tr16(a[0], a[2]); a[1] = tr16(a[1], a[3]);
  return tr32(a[0], a[1]);
}
#define VMWAIT(n) asm volatile("s_waitcnt vmcnt(%0)" :: "n"(n) : "memory")
#define LGKM0() asm volatile("s_waitcnt lgkmcnt(0)" ::: "memory")
#define MEMBAR() asm volatile("" ::: "memory")
#define LDNT(r, off, so) __builtin_amdgcn_raw_buffer_load_b128((r), (off), (so), 2)
#define LDSC(r, off, so) __builtin_amdgcn_raw_buffer_load_b128((r), (off), (so), 16)
#define LDSC8(r, off, so) __builtin_amdgcn_raw_buffer_load_b64((r), (off), (so), 16)
#define STSC(v, r, off) __builtin_amdgcn_raw_buffer_store_b128((v), (r), (off), 0, 16)
#define STSC8(v, r, off) __builtin_amdgcn_raw_buffer_store_b64((v), (r), (off), 0, 16)
#define SB() __builtin_amdgcn_sched_barrier(0)
#define PIN8(a) asm volatile("" : "+v"((a)[0]), "+v"((a)[1]), "+v"((a)[2]), "+v"((a)[3]), "+v"((a)[4]), "+v"((a)[5]), "+v"((a)[6]), "+v"((a)[7]))

__device__ __forceinline__ void layernorm9(float (&v)[9]) {
  float s = ((v[0] + v[1]) + (v[2] + v[3])) + ((v[4] + v[5]) + (v[6] + v[7])) + v[8];
  const float mean = wave_sum(s) * (1.f / D);
  float s2 = 0.f, s3 = 0.f;
#pragma unroll
  for (int k = 0; k < 8; k += 2) { const float d = v[k] - mean, e = v[k + 1] - mean; s2 = fmaf(d, d, s2); s3 = fmaf(e, e, s3); }
  { const float d = v[8] - mean; s2 = fmaf(d, d, s2); }
  const float var = rbf(wave_sum(s2 + s3) * (1.f / D));
  const float mb = rbf(mean);
  const float den = rbf(__builtin_amdgcn_sqrtf(rbf(var + 1e-5f)));
  const float inv = __builtin_amdgcn_rcpf(den);
#pragma unroll
  for (int k = 0; k < 9; ++k) {
    const float n = rbf(v[k] - mb);
    float qv = n * inv;
    qv = fmaf(fmaf(-qv, den, n), inv, qv);
    v[k] = rbf(qv);
  }
}
__device__ __forceinline__ void store9(LAS unsigned* dst, const float (&v)[9], int lane) {
  *(LAS v4u*)(dst + 4 * lane) = v4u{pkhi(asu(v[0]), asu(v[1])), pkhi(asu(v[2]), asu(v[3])), pkhi(asu(v[4]), asu(v[5])), pkhi(asu(v[6]), asu(v[7]))};
  ((LAS u16*)dst)[512 + lane] = (u16)(asu(v[8]) >> 16);
}

#define WSTAMP(k) do {} while (0)

__global__ void __launch_bounds__(NTHR) fused_decode(const u16* __restrict__ x, const u16* __restrict__ kc, const u16* __restrict__ vc,
                                                     const u16* __restrict__ wq, const u16* __restrict__ wo, const u16* __restrict__ w1,
                                                     const u16* __restrict__ b1, const u16* __restrict__ w2, const u16* __restrict__ b2,
                                                     u16* __restrict__ out, unsigned char* ws) {
  const unsigned tk0 = (unsigned)__builtin_amdgcn_s_memrealtime();
  unsigned long long pc0;
  asm volatile("s_getpc_b64 %0" : "=s"(pc0));
  extern __shared__ __attribute__((aligned(16))) float smem[];
  const int t = threadIdx.x;
  int lane = t & 63;
  int wave = __builtin_amdgcn_readfirstlane(t >> 6);
  int u = blockIdx.x;
  int g = u >> 4, gi = u & 15;
  auto mk = [](const void* p, unsigned bytes) { return __builtin_amdgcn_make_buffer_rsrc((void*)p, 0, bytes, 0x00020000); };
  const auto rws = mk(ws, WS_END);
  v4u R[18];
  v4u xa; unsigned xb, ew;
#pragma unroll
  for (int i = 0; i < 18; ++i) asm volatile("" : "=v"(R[i]));
  asm volatile("" : "=v"(xa), "=v"(xb), "=v"(ew));
  if (wave == 1) {
    const auto rx = mk(x, D * 2), rq = mk(wq, 576 * 4096 * 2);
    xa = __builtin_amdgcn_raw_buffer_load_b128(rx, lane * 16, 0, 0);
    xb = __builtin_amdgcn_raw_buffer_load_b16(rx, 1024 + lane * 2, 0, 0);
    ew = __builtin_amdgcn_raw_buffer_load_b32(rws, WS_E, 0, 16);
    const unsigned offq = ((144 * (gi & 3) + 2 * (lane >> 3)) * 4096 + 256 * g + 64 * (gi >> 2) + 8 * (lane & 7)) * 2;
#pragma unroll
    for (int i = 0; i < 18; ++i) R[i] = LDNT(rq, offq, ((i >> 1) * 16 + (i & 1)) * 8192);
  }
  int wv0 = wave;
  asm volatile("" : "+s"(wv0));
  if (wv0 == 0) {
    const auto rc = mk((const void*)(pc0 & ~255ull), CODE_KB * 1024);
    R[0] = __builtin_amdgcn_raw_buffer_load_b128(rc, ((u >> 3) % CODE_KB) * 1024 + lane * 16, 0, 0);
    if (lane < 32) ((LAS unsigned*)smem)[L_FLAG + lane] = lane == F_T0 ? tk0 : 0u;
  }
  asm volatile("" : "+s"(wv0));
  if (wv0 >= 10) {
#pragma unroll
    for (int k = 0; k < 4; ++k) *(LAS v4u*)((LAS unsigned*)smem + L_FR + 4 * ((k * 5 + (wv0 - 10)) * 64 + lane)) = v4u{0u, 0u, 0u, 0u};
  }
  unsigned kvo = ((wave - 2) & 3) * 1024 + lane * 16;
  asm volatile("" : "+v"(kvo));
  LGKM0();
  SB(); __builtin_amdgcn_s_barrier(); SB();
  if (wave >= 2 && wave < 6) {
    const auto rk = mk(kc + ((size_t)g * SK + (SK - W) + gi * 256) * 128, 65536);
    __builtin_amdgcn_s_sleep(KSLEEP);
#pragma unroll
    for (int j = 0; j < 8; ++j) R[j] = LDNT(rk, kvo, j * 4096);
  }
  SB();
  const int c = lane & 15, r = lane >> 4;
  const size_t kvb = ((size_t)g * SK + (SK - W) + gi * 256) * 128;
  const int kw = (wave - 2) & 3;
  const unsigned kvoff = kvo;
  const int lg = (u >> 5) * 8 + (u & 7);
  const bool is_red = lg < 36;
  const int rcol = 16 * lg + 4 * ((u >> 3) & 3);
  LAS float* L = (LAS float*)smem;
  LAS unsigned* LU = (LAS unsigned*)smem;
  volatile LAS unsigned* FL = (volatile LAS unsigned*)(LU + L_FLAG);
  auto wait_ge = [&](int f, unsigned v, bool sleep) {
#pragma unroll 1
    for (unsigned n = 0; n < SPIN_CAP; ++n) { if (FL[f] >= v) break; if (sleep) __builtin_amdgcn_s_sleep(1); }
    MEMBAR();
  };
  auto arrive = [&](int f) {
    MEMBAR();
    if (lane == 0) (void)__hip_atomic_fetch_add(LU + L_FLAG + f, 1u, __ATOMIC_RELAXED, __HIP_MEMORY_SCOPE_WORKGROUP);
    MEMBAR();
  };
  auto setf = [&](int f, unsigned v) { MEMBAR(); if (lane == 0) FL[f] = v; };
  auto wait_until = [&](unsigned t0, unsigned dt) {
    while ((unsigned)__builtin_amdgcn_s_memrealtime() - t0 < dt) __builtin_amdgcn_s_sleep(1);
  };
#define WDUMP() do {} while (0)
  if (wave == 1) {
      float v[9];
#pragma unroll
      for (int k = 0; k < 4; ++k) { v[2 * k] = lo16(xa[k]); v[2 * k + 1] = hi16(xa[k]); }
      v[8] = lo16(xb);
      layernorm9(v);
      store9(LU + L_XN, v, lane);
      WSTAMP(1);
      float a[8];
#pragma unroll
      for (int k = 0; k < 8; ++k) a[k] = 0.f;
#pragma unroll
      for (int i = 0; i < 9; ++i) {
        const unsigned xp = LU[L_XN + 72 * (gi & 3) + 8 * i + (lane >> 3)];
#pragma unroll
        for (int k = 0; k < 4; ++k) {
          a[2 * k] = dot2(pklo(R[2 * i][k], R[2 * i + 1][k]), xp, a[2 * k]);
          a[2 * k + 1] = dot2(pkhi(R[2 * i][k], R[2 * i + 1][k]), xp, a[2 * k + 1]);
        }
      }
      const float rr = red8(a, lane);
      const unsigned tg = __builtin_amdgcn_readfirstlane(ew) + 1u;
      const int col = 8 * (lane & 7) + ((lane >> 3) & 1) * 4 + ((lane >> 4) & 1) * 2 + (lane >> 5);
      STSC8((v2u{asu(rr), tg}), rws, (WS_Q + (u * 64 + col) * 8));
      MEMBAR();
      if (lane == 0) { FL[F_TAG] = tg; FL[F_QPUB] = 1u; }
      WSTAMP(2);
  }
  asm volatile("" : "+s"(wv0));
  if (wv0 >= 2 && wv0 < 6) {
      const auto rk = mk(kc + kvb, 65536);
      const int kq = kw;
      LAS unsigned* kt = LU + L_KT + kq * 4096;
      {
#pragma unroll
        for (int j = 8; j < 16; ++j) { VMWAIT(7); R[j] = LDNT(rk, kvo, j * 4096); }
      }
      VMWAIT(6); if (lane == 0) FL[F_K0 + kq] = 1u;
      VMWAIT(4); if (lane == 0) FL[F_K0 + kq] = 2u;
      VMWAIT(2); if (lane == 0) FL[F_K0 + kq] = 3u;
      VMWAIT(0); if (lane == 0) FL[F_K0 + kq] = 4u;
      SB();
#pragma unroll
      for (int j = 0; j < 16; ++j) *(LAS v4u*)(kt + (4 * j + r) * 64 + ((c ^ ((4 * j + r) & 15)) * 4)) = R[j];
#pragma unroll
      for (int f = 0; f < 16; ++f) R[f] = *(const LAS v4u*)(kt + (16 * (f >> 2) + c) * 64 + (((4 * (f & 3) + r) ^ c) * 4));
      PIN8(R); PIN8(R + 8);
      WSTAMP(1);
      wait_ge(F_Q, 1, false);
      WSTAMP(2);
      v4f tt[4];
#pragma unroll
      for (int T = 0; T < 4; ++T) tt[T] = v4f{0.f, 0.f, 0.f, 0.f};
#pragma unroll
      for (int k = 0; k < 4; ++k) {
        const v4u qf = *(const LAS v4u*)(LU + L_QB + (c & 1) * 64 + 16 * k + 4 * r);
#pragma unroll
        for (int T = 0; T < 4; ++T) tt[T] = mfma(qf, R[4 * T + k], tt[T]);
      }
      const float d0 = (r & 2) ? ((r & 1) ? tt[3][0] : tt[2][0]) : ((r & 1) ? tt[1][0] : tt[0][0]);
      const float d1 = (r & 2) ? ((r & 1) ? tt[3][1] : tt[2][1]) : ((r & 1) ? tt[1][1] : tt[0][1]);
      const float s0 = rbf(rbf(d0) * 0.08838834764831845f), s1 = rbf(rbf(d1) * 0.08838834764831845f);
      float m;
      {
        auto sw = __builtin_amdgcn_permlane32_swap(asu(s0), asu(s1), false, false);
        m = fmaxf(asf(sw[0]), asf(sw[1]));
        m = fmaxf(m, dpp<XOR1>(m)); m = fmaxf(m, dpp<XOR2>(m)); m = fmaxf(m, dpp<ROR4>(m)); m = fmaxf(m, dpp<XOR8>(m)); m = max16(m);
      }
      if ((lane & 31) == 0) L[L_MW + (lane >> 5) * 4 + kq] = m;
      arrive(F_MCNT);
      wait_ge(F_MCNT, 4, false);
      WSTAMP(3);
      const v4f ma = *(const LAS v4f*)(L + L_MW), mb = *(const LAS v4f*)(L + L_MW + 4);
      const float M0 = fmaxf(fmaxf(ma.x, ma.y), fmaxf(ma.z, ma.w)), M1 = fmaxf(fmaxf(mb.x, mb.y), fmaxf(mb.z, mb.w));
      const float p0 = __builtin_amdgcn_exp2f((s0 - M0) * LOG2E), p1 = __builtin_amdgcn_exp2f((s1 - M1) * LOG2E);
      const unsigned b0 = asu(rbf(p0)), b1_ = asu(rbf(p1));
      LU[L_PP + kq * 128 + (lane & 3) * 16 + (lane >> 2)] = pkhi(b0, b0);
      LU[L_PP + kq * 128 + 64 + (lane & 3) * 16 + (lane >> 2)] = pkhi(b1_, b1_);
      setf(F_P0 + kq, 1u);
      float l = tr32(p0, p1);
      l += dpp<XOR1>(l); l += dpp<XOR2>(l); l += dpp<ROR4>(l); l += dpp<XOR8>(l); l = tr16(l, l);
      if ((lane & 31) == 0) L[L_LW + (lane >> 5) * 4 + kq] = l;
      arrive(F_OCNT);
      WSTAMP(4);
  }
  asm volatile("" : "+s"(wv0));
  if (wv0 >= 2 && wv0 < 10) {
    if (wv0 >= 6) {
      v4u R[16];
      const auto rv = mk(vc + kvb, 65536);
      {
#pragma unroll
        for (int j = 0; j < 16; ++j) {
          if (j < 8 && (j & 1) == 0) wait_ge(F_K0 + kw, j / 2 + 1, true);
          if (j >= 8) { if ((unsigned)__builtin_amdgcn_s_memrealtime() - tk0 < TCATCH) VMWAIT(7); }
          R[j] = LDNT(rv, kvoff, j * 4096);
          SB();
        }
      }
      arrive(F_KVI);
      WSTAMP(1);
      v4u msk;
#pragma unroll
      for (int k = 0; k < 4; ++k) msk[k] = ((c & 7) >> 1) == k ? ((c & 1) ? 0xffff0000u : 0x0000ffffu) : 0u;
      wait_ge(F_P0 + kw, 1, true);
      WSTAMP(2);
      v4u pj[4];
#pragma unroll
      for (int k = 0; k < 4; ++k) pj[k] = *(const LAS v4u*)(LU + L_PP + kw * 128 + (c >> 3) * 64 + r * 16 + 4 * k);
      v4f acc = {0.f, 0.f, 0.f, 0.f}, acc2 = {0.f, 0.f, 0.f, 0.f};
#pragma unroll
      for (int j = 0; j < 16; ++j) {
        const unsigned pv_ = pj[j >> 2][j & 3];
        const v4u af = {msk[0] & pv_, msk[1] & pv_, msk[2] & pv_, msk[3] & pv_};
        if (j & 1) acc2 = mfma(af, R[j], acc2); else acc = mfma(af, R[j], acc);
      }
      acc += acc2;
      *(LAS v4f*)(L + L_OP + kw * 256 + (lane >> 5) * 128 + 8 * c + 4 * (r & 1)) = acc;
      arrive(F_OCNT);
      WSTAMP(4);
    }
    {
      if (is_red) {
        const int ww = (wave - 2) & 7;
        const unsigned off = (WS_P + ((32 * ww + (lane >> 1)) * 576 + rcol + 2 * (lane & 1)) * 8);
        const unsigned tag = FL[F_TAG];
        wait_ge(F_PPUB, 1, true);
        __builtin_amdgcn_s_sleep(DLY_P);
        WSTAMP(5);
        v4u pv = {0u, 0u, 0u, 0u};
#pragma unroll 1
        for (unsigned n = 0; n < POLL_CAP; ++n) {
          MEMBAR();
          pv = LDSC(rws, off, 0);
          if (__all(((pv.y ^ tag) | (pv.w ^ tag)) == 0)) break;
        }
        WSTAMP(6);
        float s0 = tr32(asf(pv.x), asf(pv.z));
        s0 += dpp<XOR2>(s0); s0 += dpp<ROR4>(s0); s0 += dpp<XOR8>(s0); s0 = tr16(s0, s0);
        if ((lane & 30) == 0) L[L_FIN + ww * 4 + 2 * (lane & 1) + (lane >> 5)] = s0;
        arrive(F_FCNT);
      }
    }
    WDUMP();
    return;
  }
  if (wv0 == 0) {
    v4u R[8];
      const auto rx = mk(x, D * 2), rb2 = mk(b2, D * 2);
      const unsigned xoff = (is_red ? (unsigned)(rcol + (lane & 3)) * 2 : OOB);
      unsigned xrr = 0u, b2r = 0u;
      { xrr = __builtin_amdgcn_raw_buffer_load_b16(rx, xoff, 0, 0); b2r = __builtin_amdgcn_raw_buffer_load_b16(rb2, xoff, 0, 0); }
      wait_ge(F_QPUB, 1, true);
      const unsigned tag = __builtin_amdgcn_readfirstlane(FL[F_TAG]);
      __builtin_amdgcn_s_sleep(DLY_Q);
      WSTAMP(1);
      {
        const unsigned qoff = WS_Q + g * 8192 + lane * 16;
#pragma unroll
        for (int k = 0; k < 8; ++k) R[k] = v4u{0u, 0u, 0u, 0u};
#pragma unroll 1
        for (unsigned n = 0; n < POLL_CAP; ++n) {
          MEMBAR();
          unsigned bad = 0;
#pragma unroll
          for (int k = 0; k < 8; ++k) R[k] = LDSC(rws, qoff, k * 1024);
#pragma unroll
          for (int k = 0; k < 8; ++k) bad |= (R[k].y ^ tag) | (R[k].w ^ tag);
          if (__all(bad == 0)) break;
        }
        float s[8];
#pragma unroll
        for (int a = 0; a < 4; ++a) { s[2 * a] = asf(R[2 * a].x) + asf(R[2 * a + 1].x); s[2 * a + 1] = asf(R[2 * a].z) + asf(R[2 * a + 1].z); }
        const float r0 = tr32(s[0], s[4]), r1_ = tr32(s[1], s[5]), r2_ = tr32(s[2], s[6]), r3 = tr32(s[3], s[7]);
        const int a0 = (lane >> 5) * 2;
        LU[L_QB + a0 * 32 + (lane & 31)] = pkhi(asu(rbf(r0)), asu(rbf(r1_)));
        LU[L_QB + (a0 + 1) * 32 + (lane & 31)] = pkhi(asu(rbf(r2_)), asu(rbf(r3)));
        setf(F_Q, 1u);
        WSTAMP(2);
      }
      wait_ge(F_OCNT, 8, false);
      WSTAMP(3);
      {
        v2f oa = *(const LAS v2f*)(L + L_OP + 2 * lane), ob = *(const LAS v2f*)(L + L_OP + 128 + 2 * lane);
#pragma unroll
        for (int w = 1; w < 4; ++w) { oa += *(const LAS v2f*)(L + L_OP + w * 256 + 2 * lane); ob += *(const LAS v2f*)(L + L_OP + w * 256 + 128 + 2 * lane); }
        const int hd = lane & 1;
        const v4f la = *(const LAS v4f*)(L + L_LW + hd * 4), ma = *(const LAS v4f*)(L + L_MW + hd * 4);
        const float Ls = (la.x + la.y) + (la.z + la.w);
        const float M = fmaxf(fmaxf(ma.x, ma.y), fmaxf(ma.z, ma.w));
        STSC((v4u{asu(oa.x), tag, asu(oa.y), tag}), rws, (WS_O + u * 2048 + lane * 16));
        STSC((v4u{asu(ob.x), tag, asu(ob.y), tag}), rws, (WS_O + u * 2048 + 1024 + lane * 16));
        STSC((v4u{asu(M), tag, asu(Ls), tag}), rws, (lane < 8 ? WS_M + u * 128 + lane * 16 : OOB));
      }
      WSTAMP(4);
      __builtin_amdgcn_s_sleep(DLY_O);
      wait_until(tk0, TPO);
      {
        const unsigned ooff = (WS_O + (16 * g + r) * 2048 + (16 * gi + c) * 8);
        const unsigned moff = (WS_M + (16 * g + r) * 128 + (gi >> 3) * 16);
        v2u ov[4]; v4u ml[4];
#pragma unroll
        for (int k = 0; k < 4; ++k) { ov[k] = v2u{0u, 0u}; ml[k] = v4u{0u, 0u, 0u, 0u}; }
#pragma unroll 1
        for (unsigned n = 0; n < POLL_CAP; ++n) {
          MEMBAR();
          unsigned bad = 0;
#pragma unroll
          for (int k = 0; k < 4; ++k) { ov[k] = LDSC8(rws, ooff, k * 8192); ml[k] = LDSC(rws, moff, k * 512); }
          if (n == 0) arrive(F_POLL);
#pragma unroll
          for (int k = 0; k < 4; ++k) bad |= (ov[k].y ^ tag) | (ml[k].y ^ tag) | (ml[k].w ^ tag);
          if (__all(bad == 0)) break;
        }
        WSTAMP(5);
        float M = fmaxf(fmaxf(asf(ml[0].x), asf(ml[1].x)), fmaxf(asf(ml[2].x), asf(ml[3].x)));
        M = max16(M); M = max32(M);
        float num = 0.f, den = 0.f;
#pragma unroll
        for (int k = 0; k < 4; ++k) { const float sc = __builtin_amdgcn_exp2f((asf(ml[k].x) - M) * LOG2E); num = fmaf(sc, asf(ov[k].x), num); den = fmaf(sc, asf(ml[k].z), den); }
        num = tr16(num, num); den = tr16(den, den); num = tr32(num, num); den = tr32(den, den);
        const float o = rbf(num * __builtin_amdgcn_rcpf(den));
        LAS u16* fp = (LAS u16*)(LU + L_FR) + ((c >> 2) * 64 + (c & 3) * 16 + 2 * r) * 8 + 2 * r;
        fp[0] = (u16)(asu(o) >> 16); fp[9] = (u16)(asu(o) >> 16);
        arrive(F_CO);
        WSTAMP(6);
      }
      if (is_red) {
        const float xb2 = lo16(xrr) + lo16(b2r);
        const auto rout = mk(out, D * 2);
        wait_ge(F_FCNT, 8, false);
        float s0 = L[L_FIN + (lane & 31)];
        s0 += dpp<ROR4>(s0); s0 += dpp<XOR8>(s0); s0 = tr16(s0, s0);
        __builtin_amdgcn_raw_buffer_store_b16((u16)(asu(rbf(s0 + xb2)) >> 16), rout, lane < 4 ? xoff : OOB, 0, 0);
        __builtin_amdgcn_raw_buffer_store_b32(tag, rws, (u == 0 && lane == 0 ? WS_E : OOB), 0, 16);
        WSTAMP(7);
      }
    WDUMP();
    return;
  }
  if (wv0 == 10) {
      const auto rb1 = mk(b1, F * 2);
      const bool hv_ok = (lane & 31) < 18;
      unsigned b1r = 0u;
      b1r = __builtin_amdgcn_raw_buffer_load_b16(rb1, hv_ok ? (unsigned)(18 * u + (lane & 31)) * 2 : OOB, 0, 2);
      wait_ge(F_QPUB, 1, true);
      const unsigned tag = __builtin_amdgcn_readfirstlane(FL[F_TAG]);
      {
        const v4u ya = *(const LAS v4u*)(LU + L_XN + 4 * lane);
        const unsigned yb = ((const LAS u16*)(LU + L_XN))[512 + lane];
        float v[9];
#pragma unroll
        for (int k = 0; k < 4; ++k) { v[2 * k] = lo16(ya[k]); v[2 * k + 1] = hi16(ya[k]); }
        v[8] = lo16(yb);
        layernorm9(v);
        store9(LU + L_Y, v, lane);
#pragma unroll
        for (int it = 0; it < 5; ++it) {
          const int idx = it * 64 + lane, n = idx < 288 ? idx >> 3 : 35, m = idx & 7;
          ((LAS u16*)(LU + L_FY))[((n >> 2) * 64 + (n & 3) * 16 + m) * 8 + m] = ((const LAS u16*)(LU + L_Y))[36 * gi + n];
        }
      }
      setf(F_Y, 1u);
      WSTAMP(1);
      const unsigned hoff = (hv_ok ? WS_H + ((16 * g + (lane >> 5)) * 288 + 18 * gi + (lane & 31)) * 8 : OOB);
      const unsigned tagx = hv_ok ? tag : 0u;
      wait_ge(F_HPUB, 3, true);
      __builtin_amdgcn_s_sleep(DLY_H);
      wait_until(__builtin_amdgcn_readfirstlane(FL[F_T0]), TPH);
      WSTAMP(2);
      v2u hv[8];
#pragma unroll
      for (int k = 0; k < 8; ++k) hv[k] = v2u{0u, 0u};
#pragma unroll 1
      for (unsigned n = 0; n < POLL_CAP; ++n) {
        MEMBAR();
        unsigned bad = 0;
#pragma unroll
        for (int k = 0; k < 8; ++k) hv[k] = LDSC8(rws, hoff, k * (2 * 288 * 8));
        if (n == 0) arrive(F_POLL);
#pragma unroll
        for (int k = 0; k < 8; ++k) bad |= hv[k].y ^ tagx;
        if (__all(bad == 0)) break;
      }
      WSTAMP(3);
      float pre = ((asf(hv[0].x) + asf(hv[1].x)) + (asf(hv[2].x) + asf(hv[3].x))) + ((asf(hv[4].x) + asf(hv[5].x)) + (asf(hv[6].x) + asf(hv[7].x)));
      pre = tr32(pre, pre);
      const float tt = rbf(rbf(pre) + lo16(b1r));
      const float inner = 0.7978845608028654f * (tt + 0.044715f * tt * tt * tt);
      const float th = 1.f - 2.f * __builtin_amdgcn_rcpf(__builtin_amdgcn_exp2f(inner * (2.f * LOG2E)) + 1.f);
      const float h = rbf(0.5f * tt * (1.f + th));
      if (hv_ok) {
        const int n = 16 + (lane & 31), m0 = 4 * (lane >> 5);
        LAS u16* fp = (LAS u16*)(LU + L_FR) + ((n >> 2) * 64 + (n & 3) * 16 + m0) * 8 + m0;
        fp[0] = (u16)(asu(h) >> 16); fp[9] = (u16)(asu(h) >> 16); fp[18] = (u16)(asu(h) >> 16); fp[27] = (u16)(asu(h) >> 16);
      }
      arrive(F_CO);
      WSTAMP(4);
    WDUMP();
    return;
  }
  {
    v4u R[18];
      const auto ro = mk(wo + (size_t)u * 16 * D, 16 * D * 2), r2 = mk(w2 + (size_t)u * 18 * D, 18 * D * 2), r1 = mk(w1, 576 * F * 2);
      const bool has1 = wave >= 11 && wave < 14;
      const unsigned tb0 = __builtin_amdgcn_readfirstlane(FL[F_T0]);
      const int cb = wave == 1 ? 512 : 128 * (wave - 11);
      const bool colok = wave != 1 || c < 8;
      const unsigned off2 = (colok ? (unsigned)(r * D + cb + 8 * c) * 2 : OOB);
      const int cl = 8 * c + 4 * (r & 1) + 2 * (r >> 1);
      {
        if (has1) {
          const unsigned off1 = c < 12 ? (unsigned)((36 * gi + r) * F + 288 * g + 96 * (wave - 11) + 8 * c) * 2 : OOB;
          wait_until(tb0, TW1); wait_ge(F_KVI, 4, true);
          WSTAMP(3);
#pragma unroll
          for (int k = 0; k < 9; ++k) R[9 + k] = LDNT(r1, off1, k * (4 * F * 2));
        }
        wait_until(tb0, TWF); wait_ge(F_KVI, 4, true);
        WSTAMP(4);
#pragma unroll
        for (int k = 0; k < 5; ++k) R[4 + k] = LDNT(r2, off2 + k * (4 * D * 2), 0);
      }
      SB();
      wait_ge(F_Y, 1, true);
      const unsigned tag = FL[F_TAG];
      if (has1) {
        v4f acc = {0.f, 0.f, 0.f, 0.f};
#pragma unroll
        for (int k = 0; k < 9; ++k) acc = mfma(*(const LAS v4u*)(LU + L_FY + k * 256 + 4 * lane), R[9 + k], acc);
        const auto s0 = __builtin_amdgcn_permlane32_swap(asu(acc[0]), asu(acc[2]), false, false);
        const auto s1 = __builtin_amdgcn_permlane32_swap(asu(acc[1]), asu(acc[3]), false, false);
        STSC((v4u{s0[0], tag, s1[0], tag}), rws, (c < 12 ? WS_H + (u * 288 + 96 * (wave - 11) + cl) * 8 : OOB));
        arrive(F_HPUB);
        WSTAMP(5);
      }
      while (FL[F_POLL] < 2u && (unsigned)__builtin_amdgcn_s_memrealtime() - tb0 < TWF2) __builtin_amdgcn_s_sleep(1);
      MEMBAR();
      {
#pragma unroll
        for (int k = 0; k < 4; ++k) R[k] = LDNT(ro, off2, k * (4 * D * 2));
      }
      SB();
      const unsigned poff = (colok ? WS_P + (u * 576 + cb + cl) * 8 : OOB);
      v4f acc = {0.f, 0.f, 0.f, 0.f}, acc2 = {0.f, 0.f, 0.f, 0.f}, acc3 = {0.f, 0.f, 0.f, 0.f};
      wait_ge(F_CO, 2, false);
#pragma unroll
      for (int k = 0; k < 9; ++k) {
        const v4u af = *(const LAS v4u*)(LU + L_FR + k * 256 + 4 * lane);
        if (k % 3 == 0) acc = mfma(af, R[k], acc); else if (k % 3 == 1) acc2 = mfma(af, R[k], acc2); else acc3 = mfma(af, R[k], acc3);
      }
      acc += acc2 + acc3;
      const auto s0 = __builtin_amdgcn_permlane32_swap(asu(acc[0]), asu(acc[2]), false, false);
      const auto s1 = __builtin_amdgcn_permlane32_swap(asu(acc[1]), asu(acc[3]), false, false);
      STSC((v4u{s0[0], tag, s1[0], tag}), rws, poff);
      setf(F_PPUB, 1u);
      WSTAMP(7);
    WDUMP();
  }
}

extern "C" void kernel_launch(void* const* d_in, const int* in_sizes, int n_in,
                              void* d_out, int out_size, void* d_ws, size_t ws_size,
                              hipStream_t stream) {
  (void)hipFuncSetAttribute((const void*)fused_decode, hipFuncAttributeMaxDynamicSharedMemorySize, LDS_BYTES);
  fused_decode<<<256, NTHR, LDS_BYTES, stream>>>((const u16*)d_in[0], (const u16*)d_in[1], (const u16*)d_in[2], (const u16*)d_in[3],
                                                 (const u16*)d_in[4], (const u16*)d_in[6], (const u16*)d_in[7], (const u16*)d_in[8],
                                                 (const u16*)d_in[9], (u16*)d_out, (unsigned char*)d_ws);
}
```

```cpp
#include <hip/hip_runtime.h>
#include <hip/hip_bf16.h>
#include <cstdint>

typedef unsigned short u16;
typedef unsigned v4u __attribute__((ext_vector_type(4)));
typedef unsigned v2u __attribute__((ext_vector_type(2)));
typedef float v4f __attribute__((ext_vector_type(4)));
typedef float v2f __attribute__((ext_vector_type(2)));
typedef __bf16 bf16x2 __attribute__((ext_vector_type(2)));
typedef short bf16x8 __attribute__((ext_vector_type(8)));
#define LAS __attribute__((address_space(3)))

#define CODE_KB 14
#define KSLEEP 10
#define DLY_Q 40
#define DLY_O 24
#define DLY_H 24
#define DLY_P 30
#define TW1 540
#define TWF 600
#define TWF2 720
#define TPO 780
#define TPH 780
#define TCATCH 480

constexpr int D = 576, SK = 65536, W = 4096, F = 4608;
constexpr int NTHR = 960;
constexpr unsigned OOB = 0x80000000u;
constexpr unsigned WS_E = 0;
constexpr unsigned WS_Q = 4096;
constexpr unsigned WS_H = WS_Q + 256 * 64 * 8;
constexpr unsigned WS_O = WS_H + 256 * 288 * 8;
constexpr unsigned WS_M = WS_O + 256 * 256 * 8;
constexpr unsigned WS_P = WS_M + 256 * 128;
constexpr unsigned WS_END = WS_P + 256 * 576 * 8;
constexpr int L_XN = 0, L_Y = L_XN + 288, L_QB = L_Y + 288, L_MW = L_QB + 128, L_LW = L_MW + 16, L_OP = L_LW + 16, L_FLAG = L_OP + 4 * 256,
              L_FR = L_FLAG + 32, L_FY = L_FR + 9 * 256, L_FIN = L_FY + 9 * 256, L_PP = L_FIN + 32, L_KT = L_PP + 512, L_END = L_KT + 16384;
constexpr int LDS_BYTES = L_END * 4;
static_assert(LDS_BYTES <= 160 * 1024 && LDS_BYTES > 80 * 1024, "one workgroup per CU");
enum { F_Y = 0, F_QPUB, F_Q, F_MCNT, F_OCNT, F_HPUB, F_CO, F_PPUB, F_TAG, F_KVI, F_FCNT, F_T0, F_POLL, F_P0 = 16, F_K0 = 20 };
constexpr unsigned SPIN_CAP = 1u << 20;
constexpr unsigned POLL_CAP = 1u << 19;
constexpr float LOG2E = 1.4426950408889634f;

__device__ __forceinline__ float rbf(float f) {
  unsigned u = __float_as_uint(f);
  u = (u + 0x7fffu + ((u >> 16) & 1u)) & 0xffff0000u;
  return __uint_as_float(u);
}
__device__ __forceinline__ float lo16(unsigned w) { return __uint_as_float(w << 16); }
__device__ __forceinline__ float hi16(unsigned w) { return __uint_as_float(w & 0xffff0000u); }
__device__ __forceinline__ float asf(unsigned w) { return __uint_as_float(w); }
__device__ __forceinline__ unsigned asu(float w) { return __float_as_uint(w); }
__device__ __forceinline__ unsigned pklo(unsigned a, unsigned b) { return __builtin_amdgcn_perm(b, a, 0x05040100u); }
__device__ __forceinline__ unsigned pkhi(unsigned a, unsigned b) { return __builtin_amdgcn_perm(b, a, 0x07060302u); }
__device__ __forceinline__ float dot2(unsigned k, unsigned q, float acc) {
  return __builtin_amdgcn_fdot2_f32_bf16(__builtin_bit_cast(bf16x2, k), __builtin_bit_cast(bf16x2, q), acc, false);
}
__device__ __forceinline__ v4f mfma(v4u a, v4u b, v4f c) {
  return __builtin_amdgcn_mfma_f32_16x16x32_bf16(__builtin_bit_cast(bf16x8, a), __builtin_bit_cast(bf16x8, b), c, 0, 0, 0);
}
template <int CTRL> __device__ __forceinline__ float dpp(float x) {
  return __builtin_bit_cast(float, __builtin_amdgcn_mov_dpp(__builtin_bit_cast(int, x), CTRL, 0xf, 0xf, true));
}
constexpr int XOR1 = 0xB1, XOR2 = 0x4E, XOR8 = 0x128, ROR4 = 0x124;
__device__ __forceinline__ float tr16(float a, float b) {
  auto s = __builtin_amdgcn_permlane16_swap(asu(a), asu(b), false, false);
  return asf(s[0]) + asf(s[1]);
}
__device__ __forceinline__ float tr32(float a, float b) {
  auto s = __builtin_amdgcn_permlane32_swap(asu(a), asu(b), false, false);
  return asf(s[0]) + asf(s[1]);
}
__device__ __forceinline__ float max16(float a) {
  auto s = __builtin_amdgcn_permlane16_swap(asu(a), asu(a), false, false);
  return fmaxf(asf(s[0]), asf(s[1]));
}
__device__ __forceinline__ float max32(float a) {
  auto s = __builtin_amdgcn_permlane32_swap(asu(a), asu(a), false, false);
  return fmaxf(asf(s[0]), asf(s[1]));
}
__device__ __forceinline__ float wave_sum(float t) {
  t += dpp<XOR1>(t); t += dpp<XOR2>(t); t += dpp<ROR4>(t); t += dpp<XOR8>(t);
  t = tr16(t, t); t = tr32(t, t);
  return t;
}
__device__ __forceinline__ float red8(float (&a)[8], int lane) {
#pragma unroll
  for (int h = 0; h < 4; ++h) { const float t1 = a[h] + dpp<XOR8>(a[h]), t2 = a[h + 4] + dpp<XOR8>(a[h + 4]); a[h] = (lane & 8) ? t2 : t1; }
  a[0] = tr16(a[0], a[2]); a[1] = tr16(a[1], a[3]);
  return tr32(a[0], a[1]);
}
#define VMWAIT(n) asm volatile("s_waitcnt vmcnt(%0)" :: "n"(n) : "memory")
#define LGKM0() asm volatile("s_waitcnt lgkmcnt(0)" ::: "memory")
#define MEMBAR() asm volatile("" ::: "memory")
#define LDNT(r, off, so) __builtin_amdgcn_raw_buffer_load_b128((r), (off), (so), 2)
#define LDSC(r, off, so) __builtin_amdgcn_raw_buffer_load_b128((r), (off), (so), 16)
#define LDSC8(r, off, so) __builtin_amdgcn_raw_buffer_load_b64((r), (off), (so), 16)
#define STSC(v, r, off) __builtin_amdgcn_raw_buffer_store_b128((v), (r), (off), 0, 16)
#define STSC8(v, r, off) __builtin_amdgcn_raw_buffer_store_b64((v), (r), (off), 0, 16)
#define SB() __builtin_amdgcn_sched_barrier(0)
#define PIN8(a) asm volatile("" : "+v"((a)[0]), "+v"((a)[1]), "+v"((a)[2]), "+v"((a)[3]), "+v"((a)[4]), "+v"((a)[5]), "+v"((a)[6]), "+v"((a)[7]))

__device__ __forceinline__ void layernorm9(float (&v)[9]) {
  float s = ((v[0] + v[1]) + (v[2] + v[3])) + ((v[4] + v[5]) + (v[6] + v[7])) + v[8];
  const float mean = wave_sum(s) * (1.f / D);
  float s2 = 0.f, s3 = 0.f;
#pragma unroll
  for (int k = 0; k < 8; k += 2) { const float d = v[k] - mean, e = v[k + 1] - mean; s2 = fmaf(d, d, s2); s3 = fmaf(e, e, s3); }
  { const float d = v[8] - mean; s2 = fmaf(d, d, s2); }
  const float var = rbf(wave_sum(s2 + s3) * (1.f / D));
  const float mb = rbf(mean);
  const float den = rbf(__builtin_amdgcn_sqrtf(rbf(var + 1e-5f)));
  const float inv = __builtin_amdgcn_rcpf(den);
#pragma unroll
  for (int k = 0; k < 9; ++k) {
    const float n = rbf(v[k] - mb);
    float qv = n * inv;
    qv = fmaf(fmaf(-qv, den, n), inv, qv);
    v[k] = rbf(qv);
  }
}
__device__ __forceinline__ void store9(LAS unsigned* dst, const float (&v)[9], int lane) {
  *(LAS v4u*)(dst + 4 * lane) = v4u{pkhi(asu(v[0]), asu(v[1])), pkhi(asu(v[2]), asu(v[3])), pkhi(asu(v[4]), asu(v[5])), pkhi(asu(v[6]), asu(v[7]))};
  ((LAS u16*)dst)[512 + lane] = (u16)(asu(v[8]) >> 16);
}

__global__ void __launch_bounds__(NTHR, 4) fused_decode(const u16* __restrict__ x, const u16* __restrict__ kc, const u16* __restrict__ vc,
                                                     const u16* __restrict__ wq, const u16* __restrict__ wo, const u16* __restrict__ w1,
                                                     const u16* __restrict__ b1, const u16* __restrict__ w2, const u16* __restrict__ b2,
                                                     u16* __restrict__ out, unsigned char* ws) {
  const unsigned tk0 = (unsigned)__builtin_amdgcn_s_memrealtime();
  unsigned long long pc0;
  asm volatile("s_getpc_b64 %0" : "=s"(pc0));
  extern __shared__ __attribute__((aligned(16))) float smem[];
  const int t = threadIdx.x;
  int lane = t & 63;
  int wave = __builtin_amdgcn_readfirstlane(t >> 6);
  int u = blockIdx.x;
  int g = u >> 4, gi = u & 15;
  auto mk = [](const void* p, unsigned bytes) { return __builtin_amdgcn_make_buffer_rsrc((void*)p, 0, bytes, 0x00020000); };
  const auto rws = mk(ws, WS_END);
  v4u R[18];
  v4u xa; unsigned xb, ew;
#pragma unroll
  for (int i = 0; i < 18; ++i) asm volatile("" : "=v"(R[i]));
  asm volatile("" : "=v"(xa), "=v"(xb), "=v"(ew));
  if (wave == 1) {
    const auto rx = mk(x, D * 2), rq = mk(wq, 576 * 4096 * 2);
    xa = __builtin_amdgcn_raw_buffer_load_b128(rx, lane * 16, 0, 0);
    xb = __builtin_amdgcn_raw_buffer_load_b16(rx, 1024 + lane * 2, 0, 0);
    ew = __builtin_amdgcn_raw_buffer_load_b32(rws, WS_E, 0, 16);
    const unsigned offq = ((144 * (gi & 3) + 2 * (lane >> 3)) * 4096 + 256 * g + 64 * (gi >> 2) + 8 * (lane & 7)) * 2;
#pragma unroll
    for (int i = 0; i < 18; ++i) R[i] = LDNT(rq, offq, ((i >> 1) * 16 + (i & 1)) * 8192);
  }
  int wv0 = wave;
  asm volatile("" : "+s"(wv0));
  if (wv0 == 0) {
    const auto rc = mk((const void*)(pc0 & ~255ull), CODE_KB * 1024);
    R[0] = __builtin_amdgcn_raw_buffer_load_b128(rc, ((u >> 3) % CODE_KB) * 1024 + lane * 16, 0, 0);
    if (lane < 32) ((LAS unsigned*)smem)[L_FLAG + lane] = lane == F_T0 ? tk0 : 0u;
  }
  asm volatile("" : "+s"(wv0));
  if (wv0 >= 10) {
#pragma unroll
    for (int k = 0; k < 4; ++k) *(LAS v4u*)((LAS unsigned*)smem + L_FR + 4 * ((k * 5 + (wv0 - 10)) * 64 + lane)) = v4u{0u, 0u, 0u, 0u};
  }
  unsigned kvo = ((wave - 2) & 3) * 1024 + lane * 16;
  asm volatile("" : "+v"(kvo));
  LGKM0();
  SB(); __builtin_amdgcn_s_barrier(); SB();
  if (wave >= 2 && wave < 6) {
    const auto rk = mk(kc + ((size_t)g * SK + (SK - W) + gi * 256) * 128, 65536);
    __builtin_amdgcn_s_sleep(KSLEEP);
#pragma unroll
    for (int j = 0; j < 8; ++j) R[j] = LDNT(rk, kvo, j * 4096);
  }
  SB();
  const int c = lane & 15, r = lane >> 4;
  const size_t kvb = ((size_t)g * SK + (SK - W) + gi * 256) * 128;
  const int kw = (wave - 2) & 3;
  const unsigned kvoff = kvo;
  const int lg = (u >> 5) * 8 + (u & 7);
  const bool is_red = lg < 36;
  const int rcol = 16 * lg + 4 * ((u >> 3) & 3);
  LAS float* L = (LAS float*)smem;
  LAS unsigned* LU = (LAS unsigned*)smem;
  volatile LAS unsigned* FL = (volatile LAS unsigned*)(LU + L_FLAG);
  auto wait_ge = [&](int f, unsigned v, bool sleep) {
#pragma unroll 1
    for (unsigned n = 0; n < SPIN_CAP; ++n) { if (FL[f] >= v) break; if (sleep) __builtin_amdgcn_s_sleep(1); }
    MEMBAR();
  };
  auto arrive = [&](int f) {
    MEMBAR();
    if (lane == 0) (void)__hip_atomic_fetch_add(LU + L_FLAG + f, 1u, __ATOMIC_RELAXED, __HIP_MEMORY_SCOPE_WORKGROUP);
    MEMBAR();
  };
  auto setf = [&](int f, unsigned v) { MEMBAR(); if (lane == 0) FL[f] = v; };
  auto wait_until = [&](unsigned t0, unsigned dt) {
    while ((unsigned)__builtin_amdgcn_s_memrealtime() - t0 < dt) __builtin_amdgcn_s_sleep(1);
  };
  if (wave == 1) {
      float v[9];
#pragma unroll
      for (int k = 0; k < 4; ++k) { v[2 * k] = lo16(xa[k]); v[2 * k + 1] = hi16(xa[k]); }
      v[8] = lo16(xb);
      layernorm9(v);
      store9(LU + L_XN, v, lane);
      MEMBAR();
      float a[8];
#pragma unroll
      for (int k = 0; k < 8; ++k) a[k] = 0.f;
#pragma unroll
      for (int i = 0; i < 9; ++i) {
        const unsigned xp = LU[L_XN + 72 * (gi & 3) + 8 * i + (lane >> 3)];
#pragma unroll
        for (int k = 0; k < 4; ++k) {
          a[2 * k] = dot2(pklo(R[2 * i][k], R[2 * i + 1][k]), xp, a[2 * k]);
          a[2 * k + 1] = dot2(pkhi(R[2 * i][k], R[2 * i + 1][k]), xp, a[2 * k + 1]);
        }
      }
      const float rr = red8(a, lane);
      const unsigned tg = __builtin_amdgcn_readfirstlane(ew) + 1u;
      const int col = 8 * (lane & 7) + ((lane >> 3) & 1) * 4 + ((lane >> 4) & 1) * 2 + (lane >> 5);
      STSC8((v2u{asu(rr), tg}), rws, (WS_Q + (u * 64 + col) * 8));
      MEMBAR();
      if (lane == 0) { FL[F_TAG] = tg; FL[F_QPUB] = 1u; }
  }
  asm volatile("" : "+s"(wv0));
  if (wv0 >= 2 && wv0 < 6) {
      const auto rk = mk(kc + kvb, 65536);
      const int kq = kw;
      LAS unsigned* kt = LU + L_KT + kq * 4096;
      {
#pragma unroll
        for (int j = 8; j < 16; ++j) { VMWAIT(7); R[j] = LDNT(rk, kvo, j * 4096); }
      }
      VMWAIT(6); if (lane == 0) FL[F_K0 + kq] = 1u;
      VMWAIT(4); if (lane == 0) FL[F_K0 + kq] = 2u;
      VMWAIT(2); if (lane == 0) FL[F_K0 + kq] = 3u;
      VMWAIT(0); if (lane == 0) FL[F_K0 + kq] = 4u;
      SB();
#pragma unroll
      for (int j = 0; j < 16; ++j) *(LAS v4u*)(kt + (4 * j + r) * 64 + ((c ^ ((4 * j + r) & 15)) * 4)) = R[j];
      MEMBAR();
#pragma unroll
      for (int f = 0; f < 16; ++f) R[f] = *(const LAS v4u*)(kt + (16 * (f >> 2) + c) * 64 + (((4 * (f & 3) + r) ^ c) * 4));
      PIN8(R); PIN8(R + 8);
      wait_ge(F_Q, 1, false);
      v4f tt[4];
#pragma unroll
      for (int T = 0; T < 4; ++T) tt[T] = v4f{0.f, 0.f, 0.f, 0.f};
#pragma unroll
      for (int k = 0; k < 4; ++k) {
        const v4u qf = *(const LAS v4u*)(LU + L_QB + (c & 1) * 64 + 16 * k + 4 * r);
#pragma unroll
        for (int T = 0; T < 4; ++T) tt[T] = mfma(qf, R[4 * T + k], tt[T]);
      }
      const float d0 = (r & 2) ? ((r & 1) ? tt[3][0] : tt[2][0]) : ((r & 1) ? tt[1][0] : tt[0][0]);
      const float d1 = (r & 2) ? ((r & 1) ? tt[3][1] : tt[2][1]) : ((r & 1) ? tt[1][1] : tt[0][1]);
      const float s0 = rbf(rbf(d0) * 0.08838834764831845f), s1 = rbf(rbf(d1) * 0.08838834764831845f);
      float m;
      {
        auto sw = __builtin_amdgcn_permlane32_swap(asu(s0), asu(s1), false, false);
        m = fmaxf(asf(sw[0]), asf(sw[1]));
        m = fmaxf(m, dpp<XOR1>(m)); m = fmaxf(m, dpp<XOR2>(m)); m = fmaxf(m, dpp<ROR4>(m)); m = fmaxf(m, dpp<XOR8>(m)); m = max16(m);
      }
      if ((lane & 31) == 0) L[L_MW + (lane >> 5) * 4 + kq] = m;
      arrive(F_MCNT);
      wait_ge(F_MCNT, 4, false);
      const v4f ma = *(const LAS v4f*)(L + L_MW), mb = *(const LAS v4f*)(L + L_MW + 4);
      const float M0 = fmaxf(fmaxf(ma.x, ma.y), fmaxf(ma.z, ma.w)), M1 = fmaxf(fmaxf(mb.x, mb.y), fmaxf(mb.z, mb.w));
      const float p0 = __builtin_amdgcn_exp2f((s0 - M0) * LOG2E), p1 = __builtin_amdgcn_exp2f((s1 - M1) * LOG2E);
      const unsigned b0 = asu(rbf(p0)), b1_ = asu(rbf(p1));
      LU[L_PP + kq * 128 + (lane & 3) * 16 + (lane >> 2)] = pkhi(b0, b0);
      LU[L_PP + kq * 128 + 64 + (lane & 3) * 16 + (lane >> 2)] = pkhi(b1_, b1_);
      setf(F_P0 + kq, 1u);
      float l = tr32(p0, p1);
      l += dpp<XOR1>(l); l += dpp<XOR2>(l); l += dpp<ROR4>(l); l += dpp<XOR8>(l); l = tr16(l, l);
      if ((lane & 31) == 0) L[L_LW + (lane >> 5) * 4 + kq] = l;
      arrive(F_OCNT);
  }
  asm volatile("" : "+s"(wv0));
  if (wv0 >= 2 && wv0 < 10) {
    if (wv0 >= 6) {
      v4u R[16];
      const auto rv = mk(vc + kvb, 65536);
      {
#pragma unroll
        for (int j = 0; j < 16; ++j) {
          if (j < 8 && (j & 1) == 0) wait_ge(F_K0 + kw, j / 2 + 1, true);
          if (j >= 8) { if ((unsigned)__builtin_amdgcn_s_memrealtime() - tk0 < TCATCH) VMWAIT(7); }
          R[j] = LDNT(rv, kvoff, j * 4096);
          SB();
        }
      }
      arrive(F_KVI);
      v4u msk;
#pragma unroll
      for (int k = 0; k < 4; ++k) msk[k] = ((c & 7) >> 1) == k ? ((c & 1) ? 0xffff0000u : 0x0000ffffu) : 0u;
      wait_ge(F_P0 + kw, 1, true);
      v4u pj[4];
#pragma unroll
      for (int k = 0; k < 4; ++k) pj[k] = *(const LAS v4u*)(LU + L_PP + kw * 128 + (c >> 3) * 64 + r * 16 + 4 * k);
      v4f acc = {0.f, 0.f, 0.f, 0.f}, acc2 = {0.f, 0.f, 0.f, 0.f};
#pragma unroll
      for (int j = 0; j < 16; ++j) {
        const unsigned pv_ = pj[j >> 2][j & 3];
        const v4u af = {msk[0] & pv_, msk[1] & pv_, msk[2] & pv_, msk[3] & pv_};
        if (j & 1) acc2 = mfma(af, R[j], acc2); else acc = mfma(af, R[j], acc);
      }
      acc += acc2;
      *(LAS v4f*)(L + L_OP + kw * 256 + (lane >> 5) * 128 + 8 * c + 4 * (r & 1)) = acc;
      arrive(F_OCNT);
    }
    {
      if (is_red) {
        const int ww = (wave - 2) & 7;
        const unsigned off = (WS_P + ((32 * ww + (lane >> 1)) * 576 + rcol + 2 * (lane & 1)) * 8);
        const unsigned tag = FL[F_TAG];
        wait_ge(F_PPUB, 1, true);
        __builtin_amdgcn_s_sleep(DLY_P);
        v4u pv = {0u, 0u, 0u, 0u};
#pragma unroll 1
        for (unsigned n = 0; n < POLL_CAP; ++n) {
          MEMBAR();
          pv = LDSC(rws, off, 0);
          if (__all(((pv.y ^ tag) | (pv.w ^ tag)) == 0)) break;
        }
        float s0 = tr32(asf(pv.x), asf(pv.z));
        s0 += dpp<XOR2>(s0); s0 += dpp<ROR4>(s0); s0 += dpp<XOR8>(s0); s0 = tr16(s0, s0);
        if ((lane & 30) == 0) L[L_FIN + ww * 4 + 2 * (lane & 1) + (lane >> 5)] = s0;
        arrive(F_FCNT);
      }
    }
    return;
  }
  if (wv0 == 0) {
    v4u R[8];
      const auto rx = mk(x, D * 2), rb2 = mk(b2, D * 2);
      const unsigned xoff = (is_red ? (unsigned)(rcol + (lane & 3)) * 2 : OOB);
      unsigned xrr = 0u, b2r = 0u;
      { xrr = __builtin_amdgcn_raw_buffer_load_b16(rx, xoff, 0, 0); b2r = __builtin_amdgcn_raw_buffer_load_b16(rb2, xoff, 0, 0); }
      wait_ge(F_QPUB, 1, true);
      const unsigned tag = __builtin_amdgcn_readfirstlane(FL[F_TAG]);
      __builtin_amdgcn_s_sleep(DLY_Q);
      {
        const unsigned qoff = WS_Q + g * 8192 + lane * 16;
#pragma unroll
        for (int k = 0; k < 8; ++k) R[k] = v4u{0u, 0u, 0u, 0u};
#pragma unroll 1
        for (unsigned n = 0; n < POLL_CAP; ++n) {
          MEMBAR();
          unsigned bad = 0;
#pragma unroll
          for (int k = 0; k < 8; ++k) R[k] = LDSC(rws, qoff, k * 1024);
#pragma unroll
          for (int k = 0; k < 8; ++k) bad |= (R[k].y ^ tag) | (R[k].w ^ tag);
          if (__all(bad == 0)) break;
        }
        float s[8];
#pragma unroll
        for (int a = 0; a < 4; ++a) { s[2 * a] = asf(R[2 * a].x) + asf(R[2 * a + 1].x); s[2 * a + 1] = asf(R[2 * a].z) + asf(R[2 * a + 1].z); }
        const float r0 = tr32(s[0], s[4]), r1_ = tr32(s[1], s[5]), r2_ = tr32(s[2], s[6]), r3 = tr32(s[3], s[7]);
        const int a0 = (lane >> 5) * 2;
        LU[L_QB + a0 * 32 + (lane & 31)] = pkhi(asu(rbf(r0)), asu(rbf(r1_)));
        LU[L_QB + (a0 + 1) * 32 + (lane & 31)] = pkhi(asu(rbf(r2_)), asu(rbf(r3)));
        setf(F_Q, 1u);
      }
      wait_ge(F_OCNT, 8, false);
      {
        v2f oa = *(const LAS v2f*)(L + L_OP + 2 * lane), ob = *(const LAS v2f*)(L + L_OP + 128 + 2 * lane);
#pragma unroll
        for (int w = 1; w < 4; ++w) { oa += *(const LAS v2f*)(L + L_OP + w * 256 + 2 * lane); ob += *(const LAS v2f*)(L + L_OP + w * 256 + 128 + 2 * lane); }
        const int hd = lane & 1;
        const v4f la = *(const LAS v4f*)(L + L_LW + hd * 4), ma = *(const LAS v4f*)(L + L_MW + hd * 4);
        const float Ls = (la.x + la.y) + (la.z + la.w);
        const float M = fmaxf(fmaxf(ma.x, ma.y), fmaxf(ma.z, ma.w));
        STSC((v4u{asu(oa.x), tag, asu(oa.y), tag}), rws, (WS_O + u * 2048 + lane * 16));
        STSC((v4u{asu(ob.x), tag, asu(ob.y), tag}), rws, (WS_O + u * 2048 + 1024 + lane * 16));
        STSC((v4u{asu(M), tag, asu(Ls), tag}), rws, (lane < 8 ? WS_M + u * 128 + lane * 16 : OOB));
      }
      __builtin_amdgcn_s_sleep(DLY_O);
      wait_until(tk0, TPO);
      {
        const unsigned ooff = (WS_O + (16 * g + r) * 2048 + (16 * gi + c) * 8);
        const unsigned moff = (WS_M + (16 * g + r) * 128 + (gi >> 3) * 16);
        v2u ov[4]; v4u ml[4];
#pragma unroll
        for (int k = 0; k < 4; ++k) { ov[k] = v2u{0u, 0u}; ml[k] = v4u{0u, 0u, 0u, 0u}; }
#pragma unroll 1
        for (unsigned n = 0; n < POLL_CAP; ++n) {
          MEMBAR();
          unsigned bad = 0;
#pragma unroll
          for (int k = 0; k < 4; ++k) { ov[k] = LDSC8(rws, ooff, k * 8192); ml[k] = LDSC(rws, moff, k * 512); }
          if (n == 0) arrive(F_POLL);
#pragma unroll
          for (int k = 0; k < 4; ++k) bad |= (ov[k].y ^ tag) | (ml[k].y ^ tag) | (ml[k].w ^ tag);
          if (__all(bad == 0)) break;
        }
        float M = fmaxf(fmaxf(asf(ml[0].x), asf(ml[1].x)), fmaxf(asf(ml[2].x), asf(ml[3].x)));
        M = max16(M); M = max32(M);
        float num = 0.f, den = 0.f;
#pragma unroll
        for (int k = 0; k < 4; ++k) { const float sc = __builtin_amdgcn_exp2f((asf(ml[k].x) - M) * LOG2E); num = fmaf(sc, asf(ov[k].x), num); den = fmaf(sc, asf(ml[k].z), den); }
        num = tr16(num, num); den = tr16(den, den); num = tr32(num, num); den = tr32(den, den);
        const float o = rbf(num * __builtin_amdgcn_rcpf(den));
        LAS u16* fp = (LAS u16*)(LU + L_FR) + ((c >> 2) * 64 + (c & 3) * 16 + 2 * r) * 8 + 2 * r;
        fp[0] = (u16)(asu(o) >> 16); fp[9] = (u16)(asu(o) >> 16);
        arrive(F_CO);
      }
      if (is_red) {
        const float xb2 = lo16(xrr) + lo16(b2r);
        const auto rout = mk(out, D * 2);
        wait_ge(F_FCNT, 8, false);
        float s0 = L[L_FIN + (lane & 31)];
        s0 += dpp<ROR4>(s0); s0 += dpp<XOR8>(s0); s0 = tr16(s0, s0);
        __builtin_amdgcn_raw_buffer_store_b16((u16)(asu(rbf(s0 + xb2)) >> 16), rout, lane < 4 ? xoff : OOB, 0, 0);
        __builtin_amdgcn_raw_buffer_store_b32(tag, rws, (u == 0 && lane == 0 ? WS_E : OOB), 0, 16);
      }
    return;
  }
  if (wv0 == 10) {
      const auto rb1 = mk(b1, F * 2);
      const bool hv_ok = (lane & 31) < 18;
      unsigned b1r = 0u;
      b1r = __builtin_amdgcn_raw_buffer_load_b16(rb1, hv_ok ? (unsigned)(18 * u + (lane & 31)) * 2 : OOB, 0, 2);
      wait_ge(F_QPUB, 1, true);
      const unsigned tag = __builtin_amdgcn_readfirstlane(FL[F_TAG]);
      {
        const v4u ya = *(const LAS v4u*)(LU + L_XN + 4 * lane);
        const unsigned yb = ((const LAS u16*)(LU + L_XN))[512 + lane];
        float v[9];
#pragma unroll
        for (int k = 0; k < 4; ++k) { v[2 * k] = lo16(ya[k]); v[2 * k + 1] = hi16(ya[k]); }
        v[8] = lo16(yb);
        layernorm9(v);
        store9(LU + L_Y, v, lane);
        MEMBAR();
#pragma unroll
        for (int it = 0; it < 5; ++it) {
          const int idx = it * 64 + lane, n = idx < 288 ? idx >> 3 : 35, m = idx & 7;
          ((LAS u16*)(LU + L_FY))[((n >> 2) * 64 + (n & 3) * 16 + m) * 8 + m] = ((const LAS u16*)(LU + L_Y))[36 * gi + n];
        }
      }
      setf(F_Y, 1u);
      const unsigned hoff = (hv_ok ? WS_H + ((16 * g + (lane >> 5)) * 288 + 18 * gi + (lane & 31)) * 8 : OOB);
      const unsigned tagx = hv_ok ? tag : 0u;
      wait_ge(F_HPUB, 3, true);
      __builtin_amdgcn_s_sleep(DLY_H);
      wait_until(__builtin_amdgcn_readfirstlane(FL[F_T0]), TPH);
      v2u hv[8];
#pragma unroll
      for (int k = 0; k < 8; ++k) hv[k] = v2u{0u, 0u};
#pragma unroll 1
      for (unsigned n = 0; n < POLL_CAP; ++n) {
        MEMBAR();
        unsigned bad = 0;
#pragma unroll
        for (int k = 0; k < 8; ++k) hv[k] = LDSC8(rws, hoff, k * (2 * 288 * 8));
        if (n == 0) arrive(F_POLL);
#pragma unroll
        for (int k = 0; k < 8; ++k) bad |= hv[k].y ^ tagx;
        if (__all(bad == 0)) break;
      }
      float pre = ((asf(hv[0].x) + asf(hv[1].x)) + (asf(hv[2].x) + asf(hv[3].x))) + ((asf(hv[4].x) + asf(hv[5].x)) + (asf(hv[6].x) + asf(hv[7].x)));
      pre = tr32(pre, pre);
      const float tt = rbf(rbf(pre) + lo16(b1r));
      const float inner = 0.7978845608028654f * (tt + 0.044715f * tt * tt * tt);
      const float th = 1.f - 2.f * __builtin_amdgcn_rcpf(__builtin_amdgcn_exp2f(inner * (2.f * LOG2E)) + 1.f);
      const float h = rbf(0.5f * tt * (1.f + th));
      if (hv_ok) {
        const int n = 16 + (lane & 31), m0 = 4 * (lane >> 5);
        LAS u16* fp = (LAS u16*)(LU + L_FR) + ((n >> 2) * 64 + (n & 3) * 16 + m0) * 8 + m0;
        fp[0] = (u16)(asu(h) >> 16); fp[9] = (u16)(asu(h) >> 16); fp[18] = (u16)(asu(h) >> 16); fp[27] = (u16)(asu(h) >> 16);
      }
      arrive(F_CO);
    return;
  }
  {
    v4u R[18];
      const auto ro = mk(wo + (size_t)u * 16 * D, 16 * D * 2), r2 = mk(w2 + (size_t)u * 18 * D, 18 * D * 2), r1 = mk(w1, 576 * F * 2);
      const bool has1 = wave >= 11 && wave < 14;
      const unsigned tb0 = __builtin_amdgcn_readfirstlane(FL[F_T0]);
      const int cb = wave == 1 ? 512 : 128 * (wave - 11);
      const bool colok = wave != 1 || c < 8;
      const unsigned off2 = (colok ? (unsigned)(r * D + cb + 8 * c) * 2 : OOB);
      const int cl = 8 * c + 4 * (r & 1) + 2 * (r >> 1);
      {
        if (has1) {
          const unsigned off1 = c < 12 ? (unsigned)((36 * gi + r) * F + 288 * g + 96 * (wave - 11) + 8 * c) * 2 : OOB;
          wait_until(tb0, TW1); wait_ge(F_KVI, 4, true);
#pragma unroll
          for (int k = 0; k < 9; ++k) R[9 + k] = LDNT(r1, off1, k * (4 * F * 2));
        }
        wait_until(tb0, TWF); wait_ge(F_KVI, 4, true);
#pragma unroll
        for (int k = 0; k < 5; ++k) R[4 + k] = LDNT(r2, off2 + k * (4 * D * 2), 0);
      }
      SB();
      wait_ge(F_Y, 1, true);
      const unsigned tag = FL[F_TAG];
      if (has1) {
        v4f acc = {0.f, 0.f, 0.f, 0.f};
#pragma unroll
        for (int k = 0; k < 9; ++k) acc = mfma(*(const LAS v4u*)(LU + L_FY + k * 256 + 4 * lane), R[9 + k], acc);
        const auto s0 = __builtin_amdgcn_permlane32_swap(asu(acc[0]), asu(acc[2]), false, false);
        const auto s1 = __builtin_amdgcn_permlane32_swap(asu(acc[1]), asu(acc[3]), false, false);
        STSC((v4u{s0[0], tag, s1[0], tag}), rws, (c < 12 ? WS_H + (u * 288 + 96 * (wave - 11) + cl) * 8 : OOB));
        arrive(F_HPUB);
      }
      while (FL[F_POLL] < 2u && (unsigned)__builtin_amdgcn_s_memrealtime() - tb0 < TWF2) __builtin_amdgcn_s_sleep(1);
      MEMBAR();
      {
#pragma unroll
        for (int k = 0; k < 4; ++k) R[k] = LDNT(ro, off2, k * (4 * D * 2));
      }
      SB();
      const unsigned poff = (colok ? WS_P + (u * 576 + cb + cl) * 8 : OOB);
      v4f acc = {0.f, 0.f, 0.f, 0.f}, acc2 = {0.f, 0.f, 0.f, 0.f}, acc3 = {0.f, 0.f, 0.f, 0.f};
      wait_ge(F_CO, 2, false);
#pragma unroll
      for (int k = 0; k < 9; ++k) {
        const v4u af = *(const LAS v4u*)(LU + L_FR + k * 256 + 4 * lane);
        if (k % 3 == 0) acc = mfma(af, R[k], acc); else if (k % 3 == 1) acc2 = mfma(af, R[k], acc2); else acc3 = mfma(af, R[k], acc3);
      }
      acc += acc2 + acc3;
      const auto s0 = __builtin_amdgcn_permlane32_swap(asu(acc[0]), asu(acc[2]), false, false);
      const auto s1 = __builtin_amdgcn_permlane32_swap(asu(acc[1]), asu(acc[3]), false, false);
      STSC((v4u{s0[0], tag, s1[0], tag}), rws, poff);
      setf(F_PPUB, 1u);
  }
}

extern "C" void kernel_launch(void* const* d_in, const int* in_sizes, int n_in,
                              void* d_out, int out_size, void* d_ws, size_t ws_size,
                              hipStream_t stream) {
  (void)hipFuncSetAttribute((const void*)fused_decode, hipFuncAttributeMaxDynamicSharedMemorySize, LDS_BYTES);
  fused_decode<<<256, NTHR, LDS_BYTES, stream>>>((const u16*)d_in[0], (const u16*)d_in[1], (const u16*)d_in[2], (const u16*)d_in[3],
                                                 (const u16*)d_in[4], (const u16*)d_in[6], (const u16*)d_in[7], (const u16*)d_in[8],
                                                 (const u16*)d_in[9], (u16*)d_out, (unsigned char*)d_ws);
}
```

```cpp
#include <hip/hip_runtime.h>
#include <hip/hip_bf16.h>
#include <cstdint>

typedef unsigned short u16;
typedef unsigned v4u __attribute__((ext_vector_type(4)));
typedef unsigned v2u __attribute__((ext_vector_type(2)));
typedef float v4f __attribute__((ext_vector_type(4)));
typedef float v2f __attribute__((ext_vector_type(2)));
typedef __bf16 bf16x2 __attribute__((ext_vector_type(2)));
typedef short bf16x8 __attribute__((ext_vector_type(8)));
#define LAS __attribute__((address_space(3)))

#define CODE_KB 14
#define KSLEEP 10
#define DLY_Q 40
#define DLY_O 24
#define DLY_H 24
#define DLY_P 30
#define TW1 560
#define TWF 620
#define TWF2 800
#define TPO 740
#define TPH 740
#define TCATCH 480

constexpr int D = 576, SK = 65536, W = 4096, F = 4608;
constexpr int NTHR = 960;
constexpr unsigned OOB = 0x80000000u;
constexpr unsigned WS_E = 0;
constexpr unsigned WS_Q = 4096;
constexpr unsigned WS_H = WS_Q + 256 * 64 * 8;
constexpr unsigned WS_O = WS_H + 256 * 288 * 8;
constexpr unsigned WS_M = WS_O + 256 * 256 * 8;
constexpr unsigned WS_P = WS_M + 256 * 128;
constexpr unsigned WS_PAD = WS_P + 256 * 576 * 8;
constexpr unsigned WS_END = WS_PAD + 4096;
constexpr int L_XN = 0, L_Y = L_XN + 288, L_QB = L_Y + 288, L_MW = L_QB + 128, L_LW = L_MW + 16, L_OP = L_LW + 16, L_FLAG = L_OP + 4 * 256,
              L_FR = L_FLAG + 32, L_FY = L_FR + 9 * 256, L_FIN = L_FY + 9 * 256, L_PP = L_FIN + 32, L_KT = L_PP + 512, L_END = L_KT + 16384;
constexpr int LDS_BYTES = L_END * 4;
static_assert(LDS_BYTES <= 160 * 1024 && LDS_BYTES > 80 * 1024, "one workgroup per CU");
enum { F_Y = 0, F_QPUB, F_Q, F_MCNT, F_OCNT, F_HPUB, F_CO, F_PPUB, F_TAG, F_KVI, F_FCNT, F_T0, F_POLL, F_P0 = 16, F_K0 = 20 };
constexpr unsigned SPIN_CAP = 1u << 25;
constexpr unsigned POLL_CAP = 1u << 19;
constexpr float LOG2E = 1.4426950408889634f;

__device__ __forceinline__ float rbf(float f) {
  unsigned u = __float_as_uint(f);
  u = (u + 0x7fffu + ((u >> 16) & 1u)) & 0xffff0000u;
  return __uint_as_float(u);
}
__device__ __forceinline__ float lo16(unsigned w) { return __uint_as_float(w << 16); }
__device__ __forceinline__ float hi16(unsigned w) { return __uint_as_float(w & 0xffff0000u); }
__device__ __forceinline__ float asf(unsigned w) { return __uint_as_float(w); }
__device__ __forceinline__ unsigned asu(float w) { return __float_as_uint(w); }
__device__ __forceinline__ unsigned pklo(unsigned a, unsigned b) { return __builtin_amdgcn_perm(b, a, 0x05040100u); }
__device__ __forceinline__ unsigned pkhi(unsigned a, unsigned b) { return __builtin_amdgcn_perm(b, a, 0x07060302u); }
__device__ __forceinline__ float dot2(unsigned k, unsigned q, float acc) {
  return __builtin_amdgcn_fdot2_f32_bf16(__builtin_bit_cast(bf16x2, k), __builtin_bit_cast(bf16x2, q), acc, false);
}
__device__ __forceinline__ v4f mfma(v4u a, v4u b, v4f c) {
  return __builtin_amdgcn_mfma_f32_16x16x32_bf16(__builtin_bit_cast(bf16x8, a), __builtin_bit_cast(bf16x8, b), c, 0, 0, 0);
}
template <int CTRL> __device__ __forceinline__ float dpp(float x) {
  return __builtin_bit_cast(float, __builtin_amdgcn_mov_dpp(__builtin_bit_cast(int, x), CTRL, 0xf, 0xf, true));
}
constexpr int XOR1 = 0xB1, XOR2 = 0x4E, XOR8 = 0x128, ROR4 = 0x124;
__device__ __forceinline__ float tr16(float a, float b) {
  auto s = __builtin_amdgcn_permlane16_swap(asu(a), asu(b), false, false);
  return asf(s[0]) + asf(s[1]);
}
__device__ __forceinline__ float tr32(float a, float b) {
  auto s = __builtin_amdgcn_permlane32_swap(asu(a), asu(b), false, false);
  return asf(s[0]) + asf(s[1]);
}
__device__ __forceinline__ float max16(float a) {
  auto s = __builtin_amdgcn_permlane16_swap(asu(a), asu(a), false, false);
  return fmaxf(asf(s[0]), asf(s[1]));
}
__device__ __forceinline__ float max32(float a) {
  auto s = __builtin_amdgcn_permlane32_swap(asu(a), asu(a), false, false);
  return fmaxf(asf(s[0]), asf(s[1]));
}
__device__ __forceinline__ float wave_sum(float t) {
  t += dpp<XOR1>(t); t += dpp<XOR2>(t); t += dpp<ROR4>(t); t += dpp<XOR8>(t);
  t = tr16(t, t); t = tr32(t, t);
  return t;
}
__device__ __forceinline__ float red8(float (&a)[8], int lane) {
#pragma unroll
  for (int h = 0; h < 4; ++h) { const float t1 = a[h] + dpp<XOR8>(a[h]), t2 = a[h + 4] + dpp<XOR8>(a[h + 4]); a[h] = (lane & 8) ? t2 : t1; }
  a[0] = tr16(a[0], a[2]); a[1] = tr16(a[1], a[3]);
  return tr32(a[0], a[1]);
}
#define VMWAIT(n) asm volatile("s_waitcnt vmcnt(%0)" :: "n"(n) : "memory")
#define LGKM0() asm volatile("s_waitcnt lgkmcnt(0)" ::: "memory")
#define MEMBAR() asm volatile("" ::: "memory")
#define LDNT(r, off, so) __builtin_amdgcn_raw_buffer_load_b128((r), (off), (so), 2)
#define LDSC(r, off, so) __builtin_amdgcn_raw_buffer_load_b128((r), (off), (so), 16)
#define LDSC8(r, off, so) __builtin_amdgcn_raw_buffer_load_b64((r), (off), (so), 16)
#define STSC(v, r, off) __builtin_amdgcn_raw_buffer_store_b128((v), (r), (off), 0, 16)
#define STSC8(v, r, off) __builtin_amdgcn_raw_buffer_store_b64((v), (r), (off), 0, 16)
#define SB() __builtin_amdgcn_sched_barrier(0)
#define PIN8(a) asm volatile("" : "+v"((a)[0]), "+v"((a)[1]), "+v"((a)[2]), "+v"((a)[3]), "+v"((a)[4]), "+v"((a)[5]), "+v"((a)[6]), "+v"((a)[7]))

__device__ __forceinline__ void layernorm9(float (&v)[9]) {
  float s = ((v[0] + v[1]) + (v[2] + v[3])) + ((v[4] + v[5]) + (v[6] + v[7])) + v[8];
  const float mean = wave_sum(s) * (1.f / D);
  float s2 = 0.f, s3 = 0.f;
#pragma unroll
  for (int k = 0; k < 8; k += 2) { const float d = v[k] - mean, e = v[k + 1] - mean; s2 = fmaf(d, d, s2); s3 = fmaf(e, e, s3); }
  { const float d = v[8] - mean; s2 = fmaf(d, d, s2); }
  const float var = rbf(wave_sum(s2 + s3) * (1.f / D));
  const float mb = rbf(mean);
  const float den = rbf(__builtin_amdgcn_sqrtf(rbf(var + 1e-5f)));
  const float inv = __builtin_amdgcn_rcpf(den);
#pragma unroll
  for (int k = 0; k < 9; ++k) {
    const float n = rbf(v[k] - mb);
    float qv = n * inv;
    qv = fmaf(fmaf(-qv, den, n), inv, qv);
    v[k] = rbf(qv);
  }
}
__device__ __forceinline__ void store9(LAS unsigned* dst, const float (&v)[9], int lane) {
  *(LAS v4u*)(dst + 4 * lane) = v4u{pkhi(asu(v[0]), asu(v[1])), pkhi(asu(v[2]), asu(v[3])), pkhi(asu(v[4]), asu(v[5])), pkhi(asu(v[6]), asu(v[7]))};
  ((LAS u16*)dst)[512 + lane] = (u16)(asu(v[8]) >> 16);
}

__global__ void __launch_bounds__(NTHR, 4) fused_decode(const u16* __restrict__ x, const u16* __restrict__ kc, const u16* __restrict__ vc,
                                                     const u16* __restrict__ wq, const u16* __restrict__ wo, const u16* __restrict__ w1,
                                                     const u16* __restrict__ b1, const u16* __restrict__ w2, const u16* __restrict__ b2,
                                                     u16* __restrict__ out, unsigned char* ws) {
  const unsigned tk0 = (unsigned)__builtin_amdgcn_s_memrealtime();
  unsigned long long pc0;
  asm volatile("s_getpc_b64 %0" : "=s"(pc0));
  extern __shared__ __attribute__((aligned(16))) float smem[];
  const int t = threadIdx.x;
  int lane = t & 63;
  int wave = __builtin_amdgcn_readfirstlane(t >> 6);
  int u = blockIdx.x;
  int g = u >> 4, gi = u & 15;
  auto mk = [](const void* p, unsigned bytes) { return __builtin_amdgcn_make_buffer_rsrc((void*)p, 0, bytes, 0x00020000); };
  const auto rws = mk(ws, WS_END);
  v4u R[18];
  v4u xa; unsigned xb, ew;
#pragma unroll
  for (int i = 0; i < 18; ++i) asm volatile("" : "=v"(R[i]));
  asm volatile("" : "=v"(xa), "=v"(xb), "=v"(ew));
  if (wave == 1) {
    const auto rx = mk(x, D * 2), rq = mk(wq, 576 * 4096 * 2);
    xa = __builtin_amdgcn_raw_buffer_load_b128(rx, lane * 16, 0, 0);
    xb = __builtin_amdgcn_raw_buffer_load_b16(rx, 1024 + lane * 2, 0, 0);
    ew = __builtin_amdgcn_raw_buffer_load_b32(rws, WS_E, 0, 16);
    const unsigned offq = ((144 * (gi & 3) + 2 * (lane >> 3)) * 4096 + 256 * g + 64 * (gi >> 2) + 8 * (lane & 7)) * 2;
#pragma unroll
    for (int i = 0; i < 18; ++i) R[i] = LDNT(rq, offq, ((i >> 1) * 16 + (i & 1)) * 8192);
  }
  int wv0 = wave;
  asm volatile("" : "+s"(wv0));
  if (wv0 == 0) {
    const auto rc = mk((const void*)(pc0 & ~255ull), CODE_KB * 1024);
    R[0] = __builtin_amdgcn_raw_buffer_load_b128(rc, ((u >> 3) % CODE_KB) * 1024 + lane * 16, 0, 0);
    if (lane < 32) ((LAS unsigned*)smem)[L_FLAG + lane] = lane == F_T0 ? tk0 : 0u;
  }
  asm volatile("" : "+s"(wv0));
  if (wv0 == 10) {
    const unsigned o1 = lane == 0 ? 0u : OOB;
    const size_t kb = ((size_t)g * SK + (SK - W) + gi * 256) * 128;
    R[0].x = __builtin_amdgcn_raw_buffer_load_b32(mk(kc + kb, 65536), o1, 0, 2);
    R[1].x = __builtin_amdgcn_raw_buffer_load_b32(mk(vc + kb, 65536), o1, 0, 2);
    R[2].x = __builtin_amdgcn_raw_buffer_load_b32(mk(w1, 576 * F * 2), lane < 2 ? (unsigned)((36 * gi + 35 * lane) * F + 288 * g) * 2 : OOB, 0, 2);
    R[3].x = __builtin_amdgcn_raw_buffer_load_b32(mk(w2 + (size_t)u * 18 * D, 18 * D * 2), o1, 0, 2);
    R[4].x = __builtin_amdgcn_raw_buffer_load_b32(mk(wo + (size_t)u * 16 * D, 16 * D * 2), o1, 0, 2);
    R[5].x = __builtin_amdgcn_raw_buffer_load_b32(rws, lane == 0 ? WS_PAD + (u & 31) * 128 : OOB, 0, 2);
  }
  asm volatile("" : "+s"(wv0));
  if (wv0 >= 10) {
#pragma unroll
    for (int k = 0; k < 4; ++k) *(LAS v4u*)((LAS unsigned*)smem + L_FR + 4 * ((k * 5 + (wv0 - 10)) * 64 + lane)) = v4u{0u, 0u, 0u, 0u};
  }
  unsigned kvo = ((wave - 2) & 3) * 1024 + lane * 16;
  asm volatile("" : "+v"(kvo));
  LGKM0();
  SB(); __builtin_amdgcn_s_barrier(); SB();
  if (wave >= 2 && wave < 6) {
    const auto rk = mk(kc + ((size_t)g * SK + (SK - W) + gi * 256) * 128, 65536);
    __builtin_amdgcn_s_sleep(KSLEEP);
#pragma unroll
    for (int j = 0; j < 8; ++j) R[j] = LDNT(rk, kvo, j * 4096);
  }
  SB();
  const int c = lane & 15, r = lane >> 4;
  const size_t kvb = ((size_t)g * SK + (SK - W) + gi * 256) * 128;
  const int kw = (wave - 2) & 3;
  const unsigned kvoff = kvo;
  const int lg = (u >> 5) * 8 + (u & 7);
  const bool is_red = lg < 36;
  const int rcol = 16 * lg + 4 * ((u >> 3) & 3);
  LAS float* L = (LAS float*)smem;
  LAS unsigned* LU = (LAS unsigned*)smem;
  volatile LAS unsigned* FL = (volatile LAS unsigned*)(LU + L_FLAG);
  auto wait_ge = [&](int f, unsigned v, bool sleep) {
#pragma unroll 1
    for (unsigned n = 0; n < SPIN_CAP; ++n) { if (FL[f] >= v) break; if (sleep) __builtin_amdgcn_s_sleep(1); }
    MEMBAR();
  };
  auto arrive = [&](int f) {
    MEMBAR();
    if (lane == 0) (void)__hip_atomic_fetch_add(LU + L_FLAG + f, 1u, __ATOMIC_RELAXED, __HIP_MEMORY_SCOPE_WORKGROUP);
    MEMBAR();
  };
  auto setf = [&](int f, unsigned v) { MEMBAR(); if (lane == 0) FL[f] = v; };
  auto wait_until = [&](unsigned t0, unsigned dt) {
    while ((unsigned)__builtin_amdgcn_s_memrealtime() - t0 < dt) __builtin_amdgcn_s_sleep(1);
  };
  if (wave == 1) {
      float v[9];
#pragma unroll
      for (int k = 0; k < 4; ++k) { v[2 * k] = lo16(xa[k]); v[2 * k + 1] = hi16(xa[k]); }
      v[8] = lo16(xb);
      layernorm9(v);
      store9(LU + L_XN, v, lane);
      MEMBAR();
      float a[8];
#pragma unroll
      for (int k = 0; k < 8; ++k) a[k] = 0.f;
#pragma unroll
      for (int i = 0; i < 9; ++i) {
        const unsigned xp = LU[L_XN + 72 * (gi & 3) + 8 * i + (lane >> 3)];
#pragma unroll
        for (int k = 0; k < 4; ++k) {
          a[2 * k] = dot2(pklo(R[2 * i][k], R[2 * i + 1][k]), xp, a[2 * k]);
          a[2 * k + 1] = dot2(pkhi(R[2 * i][k], R[2 * i + 1][k]), xp, a[2 * k + 1]);
        }
      }
      const float rr = red8(a, lane);
      const unsigned tg = __builtin_amdgcn_readfirstlane(ew) + 1u;
      const int col = 8 * (lane & 7) + ((lane >> 3) & 1) * 4 + ((lane >> 4) & 1) * 2 + (lane >> 5);
      STSC8((v2u{asu(rr), tg}), rws, (WS_Q + (u * 64 + col) * 8));
      MEMBAR();
      if (lane == 0) { FL[F_TAG] = tg; FL[F_QPUB] = 1u; }
  }
  asm volatile("" : "+s"(wv0));
  if (wv0 >= 2 && wv0 < 6) {
      const auto rk = mk(kc + kvb, 65536);
      const int kq = kw;
      LAS unsigned* kt = LU + L_KT + kq * 4096;
      {
#pragma unroll
        for (int j = 8; j < 16; ++j) { VMWAIT(7); R[j] = LDNT(rk, kvo, j * 4096); }
      }
      VMWAIT(6); if (lane == 0) FL[F_K0 + kq] = 1u;
      VMWAIT(4); if (lane == 0) FL[F_K0 + kq] = 2u;
      VMWAIT(2); if (lane == 0) FL[F_K0 + kq] = 3u;
      VMWAIT(0); if (lane == 0) FL[F_K0 + kq] = 4u;
      SB();
#pragma unroll
      for (int j = 0; j < 16; ++j) *(LAS v4u*)(kt + (4 * j + r) * 64 + ((c ^ ((4 * j + r) & 15)) * 4)) = R[j];
      MEMBAR();
#pragma unroll
      for (int f = 0; f < 16; ++f) R[f] = *(const LAS v4u*)(kt + (16 * (f >> 2) + c) * 64 + (((4 * (f & 3) + r) ^ c) * 4));
      PIN8(R); PIN8(R + 8);
      wait_ge(F_Q, 1, false);
      v4f tt[4];
#pragma unroll
      for (int T = 0; T < 4; ++T) tt[T] = v4f{0.f, 0.f, 0.f, 0.f};
#pragma unroll
      for (int k = 0; k < 4; ++k) {
        const v4u qf = *(const LAS v4u*)(LU + L_QB + (c & 1) * 64 + 16 * k + 4 * r);
#pragma unroll
        for (int T = 0; T < 4; ++T) tt[T] = mfma(qf, R[4 * T + k], tt[T]);
      }
      const float d0 = (r & 2) ? ((r & 1) ? tt[3][0] : tt[2][0]) : ((r & 1) ? tt[1][0] : tt[0][0]);
      const float d1 = (r & 2) ? ((r & 1) ? tt[3][1] : tt[2][1]) : ((r & 1) ? tt[1][1] : tt[0][1]);
      const float s0 = rbf(rbf(d0) * 0.08838834764831845f), s1 = rbf(rbf(d1) * 0.08838834764831845f);
      float m;
      {
        auto sw = __builtin_amdgcn_permlane32_swap(asu(s0), asu(s1), false, false);
        m = fmaxf(asf(sw[0]), asf(sw[1]));
        m = fmaxf(m, dpp<XOR1>(m)); m = fmaxf(m, dpp<XOR2>(m)); m = fmaxf(m, dpp<ROR4>(m)); m = fmaxf(m, dpp<XOR8>(m)); m = max16(m);
      }
      if ((lane & 31) == 0) L[L_MW + (lane >> 5) * 4 + kq] = m;
      arrive(F_MCNT);
      wait_ge(F_MCNT, 4, false);
      const v4f ma = *(const LAS v4f*)(L + L_MW), mb = *(const LAS v4f*)(L + L_MW + 4);
      const float M0 = fmaxf(fmaxf(ma.x, ma.y), fmaxf(ma.z, ma.w)), M1 = fmaxf(fmaxf(mb.x, mb.y), fmaxf(mb.z, mb.w));
      const float p0 = __builtin_amdgcn_exp2f((s0 - M0) * LOG2E), p1 = __builtin_amdgcn_exp2f((s1 - M1) * LOG2E);
      const unsigned b0 = asu(rbf(p0)), b1_ = asu(rbf(p1));
      LU[L_PP + kq * 128 + (lane & 3) * 16 + (lane >> 2)] = pkhi(b0, b0);
      LU[L_PP + kq * 128 + 64 + (lane & 3) * 16 + (lane >> 2)] = pkhi(b1_, b1_);
      setf(F_P0 + kq, 1u);
      float l = tr32(p0, p1);
      l += dpp<XOR1>(l); l += dpp<XOR2>(l); l += dpp<ROR4>(l); l += dpp<XOR8>(l); l = tr16(l, l);
      if ((lane & 31) == 0) L[L_LW + (lane >> 5) * 4 + kq] = l;
      arrive(F_OCNT);
  }
  asm volatile("" : "+s"(wv0));
  if (wv0 >= 2 && wv0 < 10) {
    if (wv0 >= 6) {
      v4u R[16];
      const auto rv = mk(vc + kvb, 65536);
      {
#pragma unroll
        for (int j = 0; j < 16; ++j) {
          if (j < 8 && (j & 1) == 0) wait_ge(F_K0 + kw, j / 2 + 1, true);
          if (j >= 8) { if ((unsigned)__builtin_amdgcn_s_memrealtime() - tk0 < TCATCH) VMWAIT(7); }
          R[j] = LDNT(rv, kvoff, j * 4096);
          SB();
        }
      }
      arrive(F_KVI);
      v4u msk;
#pragma unroll
      for (int k = 0; k < 4; ++k) msk[k] = ((c & 7) >> 1) == k ? ((c & 1) ? 0xffff0000u : 0x0000ffffu) : 0u;
      wait_ge(F_P0 + kw, 1, true);
      v4u pj[4];
#pragma unroll
      for (int k = 0; k < 4; ++k) pj[k] = *(const LAS v4u*)(LU + L_PP + kw * 128 + (c >> 3) * 64 + r * 16 + 4 * k);
      v4f acc = {0.f, 0.f, 0.f, 0.f}, acc2 = {0.f, 0.f, 0.f, 0.f};
#pragma unroll
      for (int j = 0; j < 16; ++j) {
        const unsigned pv_ = pj[j >> 2][j & 3];
        const v4u af = {msk[0] & pv_, msk[1] & pv_, msk[2] & pv_, msk[3] & pv_};
        if (j & 1) acc2 = mfma(af, R[j], acc2); else acc = mfma(af, R[j], acc);
      }
      acc += acc2;
      *(LAS v4f*)(L + L_OP + kw * 256 + (lane >> 5) * 128 + 8 * c + 4 * (r & 1)) = acc;
      arrive(F_OCNT);
    }
    {
      if (is_red) {
        const int ww = (wave - 2) & 7;
        const unsigned off = (WS_P + ((32 * ww + (lane >> 1)) * 576 + rcol + 2 * (lane & 1)) * 8);
        const unsigned tag = FL[F_TAG];
        wait_ge(F_PPUB, 1, true);
        __builtin_amdgcn_s_sleep(DLY_P);
        v4u pv = {0u, 0u, 0u, 0u};
#pragma unroll 1
        for (unsigned n = 0; n < POLL_CAP; ++n) {
          MEMBAR();
          pv = LDSC(rws, off, 0);
          if (__all(((pv.y ^ tag) | (pv.w ^ tag)) == 0)) break;
        }
        float s0 = tr32(asf(pv.x), asf(pv.z));
        s0 += dpp<XOR2>(s0); s0 += dpp<ROR4>(s0); s0 += dpp<XOR8>(s0); s0 = tr16(s0, s0);
        if ((lane & 30) == 0) L[L_FIN + ww * 4 + 2 * (lane & 1) + (lane >> 5)] = s0;
        arrive(F_FCNT);
      }
    }
    return;
  }
  if (wv0 == 0) {
    v4u R[8];
      const auto rx = mk(x, D * 2), rb2 = mk(b2, D * 2);
      const unsigned xoff = (is_red ? (unsigned)(rcol + (lane & 3)) * 2 : OOB);
      unsigned xrr = 0u, b2r = 0u;
      { xrr = __builtin_amdgcn_raw_buffer_load_b16(rx, xoff, 0, 0); b2r = __builtin_amdgcn_raw_buffer_load_b16(rb2, xoff, 0, 0); }
      wait_ge(F_QPUB, 1, true);
      const unsigned tag = __builtin_amdgcn_readfirstlane(FL[F_TAG]);
      __builtin_amdgcn_s_sleep(DLY_Q);
      {
        const unsigned qoff = WS_Q + g * 8192 + lane * 16;
#pragma unroll
        for (int k = 0; k < 8; ++k) R[k] = v4u{0u, 0u, 0u, 0u};
#pragma unroll 1
        for (unsigned n = 0; n < POLL_CAP; ++n) {
          MEMBAR();
          unsigned bad = 0;
#pragma unroll
          for (int k = 0; k < 8; ++k) R[k] = LDSC(rws, qoff, k * 1024);
#pragma unroll
          for (int k = 0; k < 8; ++k) bad |= (R[k].y ^ tag) | (R[k].w ^ tag);
          if (__all(bad == 0)) break;
        }
        float s[8];
#pragma unroll
        for (int a = 0; a < 4; ++a) { s[2 * a] = asf(R[2 * a].x) + asf(R[2 * a + 1].x); s[2 * a + 1] = asf(R[2 * a].z) + asf(R[2 * a + 1].z); }
        const float r0 = tr32(s[0], s[4]), r1_ = tr32(s[1], s[5]), r2_ = tr32(s[2], s[6]), r3 = tr32(s[3], s[7]);
        const int a0 = (lane >> 5) * 2;
        LU[L_QB + a0 * 32 + (lane & 31)] = pkhi(asu(rbf(r0)), asu(rbf(r1_)));
        LU[L_QB + (a0 + 1) * 32 + (lane & 31)] = pkhi(asu(rbf(r2_)), asu(rbf(r3)));
        setf(F_Q, 1u);
      }
      wait_ge(F_OCNT, 8, false);
      {
        v2f oa = *(const LAS v2f*)(L + L_OP + 2 * lane), ob = *(const LAS v2f*)(L + L_OP + 128 + 2 * lane);
#pragma unroll
        for (int w = 1; w < 4; ++w) { oa += *(const LAS v2f*)(L + L_OP + w * 256 + 2 * lane); ob += *(const LAS v2f*)(L + L_OP + w * 256 + 128 + 2 * lane); }
        const int hd = lane & 1;
        const v4f la = *(const LAS v4f*)(L + L_LW + hd * 4), ma = *(const LAS v4f*)(L + L_MW + hd * 4);
        const float Ls = (la.x + la.y) + (la.z + la.w);
        const float M = fmaxf(fmaxf(ma.x, ma.y), fmaxf(ma.z, ma.w));
        STSC((v4u{asu(oa.x), tag, asu(oa.y), tag}), rws, (WS_O + u * 2048 + lane * 16));
        STSC((v4u{asu(ob.x), tag, asu(ob.y), tag}), rws, (WS_O + u * 2048 + 1024 + lane * 16));
        STSC((v4u{asu(M), tag, asu(Ls), tag}), rws, (lane < 8 ? WS_M + u * 128 + lane * 16 : OOB));
      }
      __builtin_amdgcn_s_sleep(DLY_O);
      wait_until(tk0, TPO);
      {
        const unsigned ooff = (WS_O + (16 * g + r) * 2048 + (16 * gi + c) * 8);
        const unsigned moff = (WS_M + (16 * g + r) * 128 + (gi >> 3) * 16);
        v2u ov[4]; v4u ml[4];
#pragma unroll
        for (int k = 0; k < 4; ++k) { ov[k] = v2u{0u, 0u}; ml[k] = v4u{0u, 0u, 0u, 0u}; }
#pragma unroll 1
        for (unsigned n = 0; n < POLL_CAP; ++n) {
          MEMBAR();
          unsigned bad = 0;
#pragma unroll
          for (int k = 0; k < 4; ++k) { ov[k] = LDSC8(rws, ooff, k * 8192); ml[k] = LDSC(rws, moff, k * 512); }
          if (n == 0) arrive(F_POLL);
#pragma unroll
          for (int k = 0; k < 4; ++k) bad |= (ov[k].y ^ tag) | (ml[k].y ^ tag) | (ml[k].w ^ tag);
          if (__all(bad == 0)) break;
        }
        float M = fmaxf(fmaxf(asf(ml[0].x), asf(ml[1].x)), fmaxf(asf(ml[2].x), asf(ml[3].x)));
        M = max16(M); M = max32(M);
        float num = 0.f, den = 0.f;
#pragma unroll
        for (int k = 0; k < 4; ++k) { const float sc = __builtin_amdgcn_exp2f((asf(ml[k].x) - M) * LOG2E); num = fmaf(sc, asf(ov[k].x), num); den = fmaf(sc, asf(ml[k].z), den); }
        num = tr16(num, num); den = tr16(den, den); num = tr32(num, num); den = tr32(den, den);
        const float o = rbf(num * __builtin_amdgcn_rcpf(den));
        LAS u16* fp = (LAS u16*)(LU + L_FR) + ((c >> 2) * 64 + (c & 3) * 16 + 2 * r) * 8 + 2 * r;
        fp[0] = (u16)(asu(o) >> 16); fp[9] = (u16)(asu(o) >> 16);
        arrive(F_CO);
      }
      if (is_red) {
        const float xb2 = lo16(xrr) + lo16(b2r);
        const auto rout = mk(out, D * 2);
        wait_ge(F_FCNT, 8, false);
        float s0 = L[L_FIN + (lane & 31)];
        s0 += dpp<ROR4>(s0); s0 += dpp<XOR8>(s0); s0 = tr16(s0, s0);
        __builtin_amdgcn_raw_buffer_store_b16((u16)(asu(rbf(s0 + xb2)) >> 16), rout, lane < 4 ? xoff : OOB, 0, 0);
        __builtin_amdgcn_raw_buffer_store_b32(tag, rws, (u == 0 && lane == 0 ? WS_E : OOB), 0, 16);
      }
    return;
  }
  if (wv0 == 10) {
      const auto rb1 = mk(b1, F * 2);
      const bool hv_ok = (lane & 31) < 18;
      unsigned b1r = 0u;
      b1r = __builtin_amdgcn_raw_buffer_load_b16(rb1, hv_ok ? (unsigned)(18 * u + (lane & 31)) * 2 : OOB, 0, 2);
      wait_ge(F_QPUB, 1, true);
      const unsigned tag = __builtin_amdgcn_readfirstlane(FL[F_TAG]);
      {
        const v4u ya = *(const LAS v4u*)(LU + L_XN + 4 * lane);
        const unsigned yb = ((const LAS u16*)(LU + L_XN))[512 + lane];
        float v[9];
#pragma unroll
        for (int k = 0; k < 4; ++k) { v[2 * k] = lo16(ya[k]); v[2 * k + 1] = hi16(ya[k]); }
        v[8] = lo16(yb);
        layernorm9(v);
        store9(LU + L_Y, v, lane);
        MEMBAR();
#pragma unroll
        for (int it = 0; it < 5; ++it) {
          const int idx = it * 64 + lane, n = idx < 288 ? idx >> 3 : 35, m = idx & 7;
          ((LAS u16*)(LU + L_FY))[((n >> 2) * 64 + (n & 3) * 16 + m) * 8 + m] = ((const LAS u16*)(LU + L_Y))[36 * gi + n];
        }
      }
      setf(F_Y, 1u);
      const unsigned hoff = (hv_ok ? WS_H + ((16 * g + (lane >> 5)) * 288 + 18 * gi + (lane & 31)) * 8 : OOB);
      const unsigned tagx = hv_ok ? tag : 0u;
      wait_ge(F_HPUB, 3, true);
      __builtin_amdgcn_s_sleep(DLY_H);
      wait_until(__builtin_amdgcn_readfirstlane(FL[F_T0]), TPH);
      v2u hv[8];
#pragma unroll
      for (int k = 0; k < 8; ++k) hv[k] = v2u{0u, 0u};
#pragma unroll 1
      for (unsigned n = 0; n < POLL_CAP; ++n) {
        MEMBAR();
        unsigned bad = 0;
#pragma unroll
        for (int k = 0; k < 8; ++k) hv[k] = LDSC8(rws, hoff, k * (2 * 288 * 8));
        if (n == 0) arrive(F_POLL);
#pragma unroll
        for (int k = 0; k < 8; ++k) bad |= hv[k].y ^ tagx;
        if (__all(bad == 0)) break;
      }
      float pre = ((asf(hv[0].x) + asf(hv[1].x)) + (asf(hv[2].x) + asf(hv[3].x))) + ((asf(hv[4].x) + asf(hv[5].x)) + (asf(hv[6].x) + asf(hv[7].x)));
      pre = tr32(pre, pre);
      const float tt = rbf(rbf(pre) + lo16(b1r));
      const float inner = 0.7978845608028654f * (tt + 0.044715f * tt * tt * tt);
      const float th = 1.f - 2.f * __builtin_amdgcn_rcpf(__builtin_amdgcn_exp2f(inner * (2.f * LOG2E)) + 1.f);
      const float h = rbf(0.5f * tt * (1.f + th));
      if (hv_ok) {
        const int n = 16 + (lane & 31), m0 = 4 * (lane >> 5);
        LAS u16* fp = (LAS u16*)(LU + L_FR) + ((n >> 2) * 64 + (n & 3) * 16 + m0) * 8 + m0;
        fp[0] = (u16)(asu(h) >> 16); fp[9] = (u16)(asu(h) >> 16); fp[18] = (u16)(asu(h) >> 16); fp[27] = (u16)(asu(h) >> 16);
      }
      arrive(F_CO);
    return;
  }
  {
    v4u R[18];
      const auto ro = mk(wo + (size_t)u * 16 * D, 16 * D * 2), r2 = mk(w2 + (size_t)u * 18 * D, 18 * D * 2), r1 = mk(w1, 576 * F * 2);
      const bool has1 = wave >= 11 && wave < 14;
      const unsigned tb0 = __builtin_amdgcn_readfirstlane(FL[F_T0]);
      const int cb = wave == 1 ? 512 : 128 * (wave - 11);
      const bool colok = wave != 1 || c < 8;
      const unsigned off2 = (colok ? (unsigned)(r * D + cb + 8 * c) * 2 : OOB);
      const int cl = 8 * c + 4 * (r & 1) + 2 * (r >> 1);
      {
        if (has1) {
          const unsigned off1 = c < 12 ? (unsigned)((36 * gi + r) * F + 288 * g + 96 * (wave - 11) + 8 * c) * 2 : OOB;
          wait_until(tb0, TW1); wait_ge(F_KVI, 4, true);
#pragma unroll
          for (int k = 0; k < 9; ++k) R[9 + k] = LDNT(r1, off1, k * (4 * F * 2));
        }
        wait_until(tb0, TWF); wait_ge(F_KVI, 4, true);
#pragma unroll
        for (int k = 0; k < 5; ++k) R[4 + k] = LDNT(r2, off2 + k * (4 * D * 2), 0);
      }
      SB();
      wait_ge(F_Y, 1, true);
      const unsigned tag = FL[F_TAG];
      if (has1) {
        v4f acc = {0.f, 0.f, 0.f, 0.f};
#pragma unroll
        for (int k = 0; k < 9; ++k) acc = mfma(*(const LAS v4u*)(LU + L_FY + k * 256 + 4 * lane), R[9 + k], acc);
        const auto s0 = __builtin_amdgcn_permlane32_swap(asu(acc[0]), asu(acc[2]), false, false);
        const auto s1 = __builtin_amdgcn_permlane32_swap(asu(acc[1]), asu(acc[3]), false, false);
        STSC((v4u{s0[0], tag, s1[0], tag}), rws, (c < 12 ? WS_H + (u * 288 + 96 * (wave - 11) + cl) * 8 : OOB));
        arrive(F_HPUB);
      }
      while (FL[F_POLL] < 2u && (unsigned)__builtin_amdgcn_s_memrealtime() - tb0 < TWF2) __builtin_amdgcn_s_sleep(1);
      MEMBAR();
      {
#pragma unroll
        for (int k = 0; k < 4; ++k) R[k] = LDNT(ro, off2, k * (4 * D * 2));
      }
      SB();
      const unsigned poff = (colok ? WS_P + (u * 576 + cb + cl) * 8 : OOB);
      v4f acc = {0.f, 0.f, 0.f, 0.f}, acc2 = {0.f, 0.f, 0.f, 0.f}, acc3 = {0.f, 0.f, 0.f, 0.f};
      wait_ge(F_CO, 2, false);
#pragma unroll
      for (int k = 0; k < 9; ++k) {
        const v4u af = *(const LAS v4u*)(LU + L_FR + k * 256 + 4 * lane);
        if (k % 3 == 0) acc = mfma(af, R[k], acc); else if (k % 3 == 1) acc2 = mfma(af, R[k], acc2); else acc3 = mfma(af, R[k], acc3);
      }
      acc += acc2 + acc3;
      const auto s0 = __builtin_amdgcn_permlane32_swap(asu(acc[0]), asu(acc[2]), false, false);
      const auto s1 = __builtin_amdgcn_permlane32_swap(asu(acc[1]), asu(acc[3]), false, false);
      STSC((v4u{s0[0], tag, s1[0], tag}), rws, poff);
      setf(F_PPUB, 1u);
  }
}

extern "C" void kernel_launch(void* const* d_in, const int* in_sizes, int n_in,
                              void* d_out, int out_size, void* d_ws, size_t ws_size,
                              hipStream_t stream) {
  (void)hipFuncSetAttribute((const void*)fused_decode, hipFuncAttributeMaxDynamicSharedMemorySize, LDS_BYTES);
  fused_decode<<<256, NTHR, LDS_BYTES, stream>>>((const u16*)d_in[0], (const u16*)d_in[1], (const u16*)d_in[2], (const u16*)d_in[3],
                                                 (const u16*)d_in[4], (const u16*)d_in[6], (const u16*)d_in[7], (const u16*)d_in[8],
                                                 (const u16*)d_in[9], (u16*)d_out, (unsigned char*)d_ws);
}
```
